```python
import torch

def _softmax(x, axis=-1):
    return torch.softmax(x, dim=axis)
def _gelu(x):
    return torch.nn.functional.gelu(x, approximate="tanh")
def _silu(x):
    return torch.nn.functional.silu(x)
def _sigmoid(x):
    return torch.sigmoid(x)
def _layernorm(x, axis=-1, eps=1e-5):
    m = x.mean(dim=axis, keepdim=True)
    v = x.var(dim=axis, keepdim=True, unbiased=False)
    return (x - m) / torch.sqrt(v + eps)
def _scatter_add(tgt, idx, src, axis=0):
    return tgt.clone().index_add_(axis, idx, src)
def _segment_sum(src, seg, n):
    out = torch.zeros((n,) + src.shape[1:], dtype=src.dtype)
    return out.index_add_(0, seg, src)
def _pad(x, pw):
    flat = tuple(v for pair in reversed(pw) for v in pair)
    return torch.nn.functional.pad(x, flat)

kernel_name = 'standalone__gqa_softmax__near_ridge'

def setup_inputs():

    _w = (lambda a: (lambda t: t.to(torch.bfloat16) if t.is_floating_point() else t)(torch.as_tensor(a)))
    B, Hq, Hkv, Sq, Sk, D = 32, 32, 4, 192, 512, 128
    return {
        "q": _w((lambda *s: torch.randn(*s, dtype=torch.bfloat16) * 0.8)(B, Hq, Sq, D)),
        "k": _w((lambda *s: torch.randn(*s, dtype=torch.bfloat16) * 0.8)(B, Hkv, Sk, D)),
        "v": _w((lambda *s: torch.randn(*s, dtype=torch.bfloat16) * 0.05)(B, Hkv, Sk, D)),
    }

def reference(q, k, v):
    B, Hq, Sq, D = q.shape
    Hkv = k.shape[1]
    rep = Hq // Hkv
    k = (lambda x, n, axis: torch.repeat_interleave(x, n, dim=axis))(k, rep, axis=1)
    v = (lambda x, n, axis: torch.repeat_interleave(x, n, dim=axis))(v, rep, axis=1)
    scale = D ** -0.5
    scores = torch.einsum("bhqd,bhkd->bhqk", q, k) * scale
    p = _softmax(scores, axis=-1)
    return torch.einsum("bhqk,bhkd->bhqd", p, v)
```

```cpp
#include <hip/hip_runtime.h>
#include <hip/hip_bf16.h>
#include <cstdio>

using bf16 = __hip_bfloat16;
constexpr int   D = 128, NW = 8, QBLK = 32, KVBLK = 64;
constexpr float SCALE = 0.088388347648318440f;
#define ATTN_THR 8.f
constexpr float THR = ATTN_THR;
#define ATTN_LAYOUT 0
constexpr int LAYOUT = ATTN_LAYOUT;
#define ATTN_SDEPTH 2
constexpr int SDEPTH = ATTN_SDEPTH;
static_assert(SDEPTH == 1 || SDEPTH == 2, "SDEPTH is 1 or 2");
#define ATTN_B 32
#define ATTN_HQ 32
#define ATTN_HKV 4
#define ATTN_S 192
#define ATTN_SKV 512
constexpr int B = ATTN_B, HQ = ATTN_HQ, HKV = ATTN_HKV, S = ATTN_S, SKV = ATTN_SKV;
constexpr int LDQ = LAYOUT ? HQ * D : D, LDK = LAYOUT ? HKV * D : D, LDO = LDQ;
static_assert(SKV % (2 * KVBLK) == 0 && HQ % HKV == 0, "S % 256 == 0, SKV % 128 == 0 (even tile count), HQ % HKV == 0");
constexpr size_t SHM_V = KVBLK * D * 2, SHM_K = KVBLK * D * 2, SHM_ATTN = 2 * SHM_V + 2 * SHM_K + NW * 64 * 4;

using bf16x8 = __attribute__((ext_vector_type(8))) short;
using s16x4  = __attribute__((ext_vector_type(4))) short;
using f32x16 = __attribute__((ext_vector_type(16))) float;
using f32x8  = __attribute__((ext_vector_type(8))) float;
using u32x4  = __attribute__((ext_vector_type(4))) unsigned;
#define KSWZ(row, colB) ((row) * 256 + ((colB) ^ (((row) & 7) << 4)))
#define SBAR() __builtin_amdgcn_sched_barrier(0)
__device__ __forceinline__ int crow(int r, int hi) { return (r & 3) + 8 * (r >> 2) + 4 * hi; }
__device__ __forceinline__ unsigned cvtpk(float lo, float hi) {
  unsigned r; asm volatile("v_cvt_pk_bf16_f32 %0, %1, %2" : "=v"(r) : "v"(lo), "v"(hi)); return r;
}
template <typename TIn> struct Stage;
template <> struct Stage<bf16>  { using T = bf16x8;
  __device__ static __forceinline__ T ld8(const bf16* p) { return *reinterpret_cast<const bf16x8*>(p); }
  __device__ static __forceinline__ bf16x8 tobf(T x) { return x; } };
template <> struct Stage<float> { using T = f32x8;
  __device__ static __forceinline__ T ld8(const float* p) { return *reinterpret_cast<const f32x8*>(p); }
  __device__ static __forceinline__ bf16x8 tobf(T x) {
    u32x4 w = {cvtpk(x[0], x[1]), cvtpk(x[2], x[3]), cvtpk(x[4], x[5]), cvtpk(x[6], x[7])}; return *reinterpret_cast<bf16x8*>(&w); } };

__device__ __forceinline__ void partialSM(f32x16& p0, f32x16& p1, float& m_reg, float& mn, float& alpha) {
  constexpr float C = SCALE * 1.4426950408889634f;
  float pmax = p0[0]; for (int r = 1; r < 16; ++r) pmax = fmaxf(pmax, p0[r]); for (int r = 0; r < 16; ++r) pmax = fmaxf(pmax, p1[r]);
  { auto rr = __builtin_amdgcn_permlane32_swap(__float_as_uint(pmax), __float_as_uint(pmax), false, false);
    pmax = fmaxf(__uint_as_float(rr[0]), __uint_as_float(rr[1])); }
  if (__builtin_expect(__all(pmax - m_reg <= THR / SCALE), 1)) { mn = m_reg; alpha = 1.f; }
  else { mn = fmaxf(m_reg, pmax); alpha = __builtin_amdgcn_exp2f((m_reg - mn) * C); m_reg = mn; }
  float mnC = -mn * C;
  for (int r = 0; r < 16; ++r) p0[r] = fmaf(p0[r], C, mnC); for (int r = 0; r < 16; ++r) p1[r] = fmaf(p1[r], C, mnC);
  for (int r = 0; r < 16; ++r) p0[r] = __builtin_amdgcn_exp2f(p0[r]);
}
__device__ __forceinline__ void finishSM(f32x16& p0, f32x16& p1, float alpha, float& l_reg, bf16x8& pa0, bf16x8& pa1, bf16x8& pa2, bf16x8& pa3) {
  for (int r = 0; r < 16; ++r) p1[r] = __builtin_amdgcn_exp2f(p1[r]);
  float ps = 0; for (int r = 0; r < 16; ++r) ps += p0[r]; for (int r = 0; r < 16; ++r) ps += p1[r];
  { auto rr = __builtin_amdgcn_permlane32_swap(__float_as_uint(ps), __float_as_uint(ps), false, false);
    ps = __uint_as_float(rr[0]) + __uint_as_float(rr[1]); }
  l_reg = l_reg * alpha + ps;
#define PK4(P, BASE, OUT) do { unsigned a0 = cvtpk(P[BASE + 0], P[BASE + 1]), a1 = cvtpk(P[BASE + 2], P[BASE + 3]);   \
    unsigned b0 = cvtpk(P[BASE + 4], P[BASE + 5]), b1 = cvtpk(P[BASE + 6], P[BASE + 7]);                              \
    auto r0 = __builtin_amdgcn_permlane32_swap(a0, b0, false, false); auto r1 = __builtin_amdgcn_permlane32_swap(a1, b1, false, false); \
    u32x4 w = {r0[0], r1[0], r0[1], r1[1]}; OUT = *reinterpret_cast<bf16x8*>(&w); } while (0)
  PK4(p0, 0, pa0); PK4(p0, 8, pa1); PK4(p1, 0, pa2); PK4(p1, 8, pa3);
#undef PK4
}
__device__ __forceinline__ void qkt(f32x16& p0, f32x16& p1, const bf16* Ks, const bf16x8* qr, int r32, int hi) {
  p0 = f32x16{}; p1 = f32x16{};
  for (int d0 = 0; d0 < 8; ++d0) { int cb = (d0 * 16 + hi * 8) * 2;
    bf16x8 b0 = *reinterpret_cast<const bf16x8*>((const char*)Ks + KSWZ(r32, cb));
    bf16x8 b1 = *reinterpret_cast<const bf16x8*>((const char*)Ks + KSWZ(32 + r32, cb));
    p0 = __builtin_amdgcn_mfma_f32_32x32x16_bf16(b0, qr[d0], p0, 0, 0, 0);
    p1 = __builtin_amdgcn_mfma_f32_32x32x16_bf16(b1, qr[d0], p1, 0, 0, 0); }
}
__device__ __forceinline__ int v_st(int k, int c) { const int kk = (k & ~0xC) | ((k & 4) << 1) | ((k & 8) >> 1); return ((kk >> 3) * 4 + (c >> 5)) * 512 + ((kk & 7) * 32 + (c & 31)) * 2; }
__device__ __forceinline__ int v_rd_base(int lane) { return ((lane & 3) << 3) | (((lane >> 2) & 3) << 6) | (((lane >> 4) & 1) << 5) | (((lane >> 5) & 1) << 8); }
constexpr int v_rd_off(int d0, int ks, int half) { return d0 * 512 + ks * 4096 + half * 2048; }
template <int OFF> __device__ __forceinline__ s16x4 tr_read(int vb) {
  s16x4 r; asm volatile("ds_read_b64_tr_b16 %0, %1 offset:%2" : "=&v"(r) : "v"(vb), "i"(OFF) : "memory"); return r;
}
template <int D0> __device__ __forceinline__ void pv_one(f32x16& od, int vb, bf16x8 pa0, bf16x8 pa1, bf16x8 pa2, bf16x8 pa3) {
  const s16x4 l0 = tr_read<v_rd_off(D0, 0, 0)>(vb), h0 = tr_read<v_rd_off(D0, 0, 1)>(vb), l1 = tr_read<v_rd_off(D0, 1, 0)>(vb), h1 = tr_read<v_rd_off(D0, 1, 1)>(vb);
  const s16x4 l2 = tr_read<v_rd_off(D0, 2, 0)>(vb), h2 = tr_read<v_rd_off(D0, 2, 1)>(vb), l3 = tr_read<v_rd_off(D0, 3, 0)>(vb), h3 = tr_read<v_rd_off(D0, 3, 1)>(vb);
  asm volatile("s_waitcnt lgkmcnt(0)" ::: "memory"); SBAR();
#define PK(L, H) (bf16x8){L[0], L[1], L[2], L[3], H[0], H[1], H[2], H[3]}
  od = __builtin_amdgcn_mfma_f32_32x32x16_bf16(pa0, PK(l0, h0), od, 0, 0, 0);
  od = __builtin_amdgcn_mfma_f32_32x32x16_bf16(pa1, PK(l1, h1), od, 0, 0, 0);
  od = __builtin_amdgcn_mfma_f32_32x32x16_bf16(pa2, PK(l2, h2), od, 0, 0, 0);
  od = __builtin_amdgcn_mfma_f32_32x32x16_bf16(pa3, PK(l3, h3), od, 0, 0, 0);
#undef PK
}
__device__ __forceinline__ void pv_d0(f32x16* o, int vb, bf16x8 pa0, bf16x8 pa1, bf16x8 pa2, bf16x8 pa3) {
  pv_one<0>(o[0], vb, pa0, pa1, pa2, pa3); pv_one<1>(o[1], vb, pa0, pa1, pa2, pa3); pv_one<2>(o[2], vb, pa0, pa1, pa2, pa3); pv_one<3>(o[3], vb, pa0, pa1, pa2, pa3);
}

template <typename TQ>
__device__ __forceinline__ void attn_dense_body(const TQ* __restrict__ Qb, const bf16* __restrict__ Kh, const bf16* __restrict__ Vh,
                                                bf16* __restrict__ Ob, int seq, char* lds) {
  using St = Stage<bf16>; using SQ = Stage<TQ>;
  const int tid = threadIdx.x, wid = tid >> 6, lane = tid & 63, r32 = lane & 31, hi = lane >> 5;
  bf16* V_lds = (bf16*)lds; bf16* K_lds = (bf16*)(lds + 2 * SHM_V);
  float* ws = (float*)(lds + 2 * SHM_V + 2 * SHM_K) + wid * 64; float* li_l = ws; float* al_l = ws + 32;
  float m_reg = -1e30f, l_reg = 0; f32x16 o[4] = {}; bf16x8 qr[8];
  const TQ* Qw = Qb + (long)(wid * QBLK + r32) * LDQ + hi * 8;
#pragma unroll
  for (int d0 = 0; d0 < 8; ++d0) qr[d0] = SQ::tobf(SQ::ld8(Qw + d0 * 16));
  const int sr = tid >> 4, sc = (tid & 15) * 8, vst0 = v_st(sr, sc), vst1 = v_st(32 + sr, sc);
  const int vb0 = (int)(uintptr_t)V_lds + v_rd_base(lane);
  struct { typename St::T vs0, vs1, ks0, ks1; } sr_[SDEPTH];
#define SLOAD(i, k0) do { sr_[i].vs0 = St::ld8(&Vh[(long)((k0) + sr) * LDK + sc]); sr_[i].vs1 = St::ld8(&Vh[(long)((k0) + 32 + sr) * LDK + sc]); \
    sr_[i].ks0 = St::ld8(&Kh[(long)((k0) + sr) * LDK + sc]); sr_[i].ks1 = St::ld8(&Kh[(long)((k0) + 32 + sr) * LDK + sc]); } while (0)
#define SWRITE(b, i) do { *(bf16x8*)((char*)V_lds + (b) * SHM_V + vst0) = St::tobf(sr_[i].vs0);          \
    *(bf16x8*)((char*)V_lds + (b) * SHM_V + vst1) = St::tobf(sr_[i].vs1); int kc = sc * 2;               \
    *(bf16x8*)((char*)K_lds + (b) * SHM_K + KSWZ(sr, kc)) = St::tobf(sr_[i].ks0);                       \
    *(bf16x8*)((char*)K_lds + (b) * SHM_K + KSWZ(32 + sr, kc)) = St::tobf(sr_[i].ks1); } while (0)
#define SWAIT() do { if constexpr (SDEPTH == 2) asm volatile("s_waitcnt vmcnt(4)" ::: "memory"); else asm volatile("s_waitcnt vmcnt(0)" ::: "memory"); } while (0)
#define RESC(a) do { if (__any((a) < 1.f)) { if (hi == 0) al_l[r32] = (a); asm volatile("s_waitcnt lgkmcnt(0)" ::: "memory"); \
    for (int d = 0; d < 4; ++d) for (int r = 0; r < 16; ++r) o[d][r] *= al_l[crow(r, hi)]; } } while (0)
  f32x16 pA0, pA1, pB0, pB1; float mnA, mnB, alA, alB; bf16x8 pa0, pa1, pa2, pa3; const int NT = seq / KVBLK;
  constexpr int SE = 0, SO = SDEPTH - 1;
  SLOAD(SE, 0); asm volatile("s_waitcnt vmcnt(0)" ::: "memory"); SWRITE(0, SE); __syncthreads();
  qkt(pA0, pA1, K_lds, qr, r32, hi); partialSM(pA0, pA1, m_reg, mnA, alA);
  SLOAD(SO, KVBLK); if constexpr (SDEPTH == 2) { if (2 < NT) SLOAD(SE, 2 * KVBLK); }
  SWAIT(); SWRITE(1, SO); __syncthreads();
  for (int j = 1; j + 1 < NT; j += 2) {
    SBAR(); qkt(pB0, pB1, (bf16*)((char*)K_lds + SHM_K), qr, r32, hi);
    finishSM(pA0, pA1, alA, l_reg, pa0, pa1, pa2, pa3); SBAR();
    SLOAD(SO, (j + SDEPTH) * KVBLK); SBAR();
    pv_d0(o, vb0, pa0, pa1, pa2, pa3); partialSM(pB0, pB1, m_reg, mnB, alB);
    __syncthreads(); SWAIT(); SWRITE(0, SE);
    RESC(alB); __syncthreads();
    SBAR(); qkt(pA0, pA1, K_lds, qr, r32, hi);
    finishSM(pB0, pB1, alB, l_reg, pa0, pa1, pa2, pa3); SBAR();
    if (SDEPTH == 1 || j + 3 < NT) SLOAD(SE, (j + 1 + SDEPTH) * KVBLK); SBAR();
    pv_d0(o, vb0 + (int)SHM_V, pa0, pa1, pa2, pa3); partialSM(pA0, pA1, m_reg, mnA, alA);
    __syncthreads(); SWAIT(); SWRITE(1, SO);
    RESC(alA); __syncthreads();
  }
  SBAR(); qkt(pB0, pB1, (bf16*)((char*)K_lds + SHM_K), qr, r32, hi);
  finishSM(pA0, pA1, alA, l_reg, pa0, pa1, pa2, pa3); SBAR();
  pv_d0(o, vb0, pa0, pa1, pa2, pa3); partialSM(pB0, pB1, m_reg, mnB, alB);
  __syncthreads(); RESC(alB);
  finishSM(pB0, pB1, alB, l_reg, pa0, pa1, pa2, pa3); SBAR();
  pv_d0(o, vb0 + (int)SHM_V, pa0, pa1, pa2, pa3);
  if (hi == 0) li_l[r32] = l_reg; asm volatile("s_waitcnt lgkmcnt(0)" ::: "memory");
  float rli[16];
#pragma unroll
  for (int r = 0; r < 16; ++r) rli[r] = __builtin_amdgcn_rcpf(li_l[crow(r, hi)]);
  bf16* Ow = Ob + (long)(wid * QBLK) * LDO;
#pragma unroll
  for (int r = 0; r < 16; ++r) { int orow = crow(r, hi);
    for (int d0 = 0; d0 < 4; ++d0) Ow[(long)orow * LDO + d0 * 32 + r32] = __float2bfloat16(o[d0][r] * rli[r]); }
#undef SLOAD
#undef SWRITE
#undef SWAIT
#undef RESC
}

__global__ __launch_bounds__(64 * NW, 1)
void attn_fwd_dense(const bf16* __restrict__ Q, const bf16* __restrict__ K, const bf16* __restrict__ V, bf16* __restrict__ O) {
  extern __shared__ __attribute__((aligned(16))) char shm[];
  const int bid = blockIdx.x;
  const int xcd = bid & 7, j = bid >> 3;
  const int g = xcd * 16 + j / 6, blk = j % 6;
  const long q0 = ((long)g * 1536 + blk * 256) * D, k0 = (long)g * 512 * D;
  attn_dense_body<bf16>(Q + q0, K + k0, V + k0, O + q0, 512, shm);
}
extern "C" void kernel_launch(void* const* d_in, const int* in_sizes, int n_in, void* d_out, int out_size, void* d_ws, size_t ws_size, hipStream_t stream) {
  hipFuncSetAttribute((const void*)attn_fwd_dense, hipFuncAttributeMaxDynamicSharedMemorySize, (int)SHM_ATTN);
  hipLaunchKernelGGL(attn_fwd_dense, dim3(768), dim3(64 * NW), SHM_ATTN, stream,
                     (const bf16*)d_in[0], (const bf16*)d_in[1], (const bf16*)d_in[2], (bf16*)d_out);
}
```

```cpp
#include <hip/hip_runtime.h>
#include <hip/hip_bf16.h>

typedef __attribute__((ext_vector_type(8))) short  bf16x8;
typedef __attribute__((ext_vector_type(4))) float  f32x4;
typedef __attribute__((ext_vector_type(2))) float  f32x2;
typedef __attribute__((ext_vector_type(2))) __bf16 bf16x2;

__device__ __forceinline__ unsigned lane_id() {
  unsigned l; asm volatile("v_mbcnt_lo_u32_b32 %0, -1, 0\n\tv_mbcnt_hi_u32_b32 %0, -1, %0" : "=v"(l)); return l;
}
__device__ __forceinline__ unsigned pack2(float a, float b) {
  f32x2 v = {a, b}; bf16x2 r = __builtin_convertvector(v, bf16x2); return __builtin_bit_cast(unsigned, r);
}

__device__ __forceinline__ void exact_rows(const unsigned short* __restrict__ Q, const unsigned short* __restrict__ K,
                                        const unsigned short* __restrict__ V, unsigned short* __restrict__ O, long qrow) {
  const unsigned l = lane_id(), i = l & 15u, g = l >> 4;
  const float C = 0.12751743082459868f;
  for (int qb = 0; qb < 6; ++qb) {
    const long row = qrow + 16 * qb + i;
    bf16x8 qf[4];
#pragma unroll
    for (int s = 0; s < 4; ++s) qf[s] = *reinterpret_cast<const bf16x8*>(Q + row * 128 + 32 * s + 8 * g);
    float m = -INFINITY;
    for (int kb = 0; kb < 32; ++kb) {
      f32x4 sc = {0.f, 0.f, 0.f, 0.f};
#pragma unroll
      for (int s = 0; s < 4; ++s)
        sc = __builtin_amdgcn_mfma_f32_16x16x32_bf16(*reinterpret_cast<const bf16x8*>(K + (16 * kb + i) * 128 + 32 * s + 8 * g), qf[s], sc, 0, 0, 0);
      m = fmaxf(fmaxf(m, fmaxf(sc[0], sc[1])), fmaxf(sc[2], sc[3]));
    }
    m = fmaxf(m, __shfl_xor(m, 16)); m = fmaxf(m, __shfl_xor(m, 32));
    f32x4 o[8];
#pragma unroll
    for (int db = 0; db < 8; ++db) o[db] = f32x4{0.f, 0.f, 0.f, 0.f};
    float lsum = 0.f;
    for (int ks = 0; ks < 16; ++ks) {
      float p[8];
#pragma unroll
      for (int h = 0; h < 2; ++h) {
        f32x4 sc = {0.f, 0.f, 0.f, 0.f};
#pragma unroll
        for (int s = 0; s < 4; ++s)
          sc = __builtin_amdgcn_mfma_f32_16x16x32_bf16(*reinterpret_cast<const bf16x8*>(K + (32 * ks + 16 * h + i) * 128 + 32 * s + 8 * g), qf[s], sc, 0, 0, 0);
#pragma unroll
        for (int r = 0; r < 4; ++r) { p[4 * h + r] = __builtin_amdgcn_exp2f((sc[r] - m) * C); lsum += p[4 * h + r]; }
      }
      typedef __attribute__((ext_vector_type(4))) unsigned u32x4;
      u32x4 pw = {pack2(p[0], p[1]), pack2(p[2], p[3]), pack2(p[4], p[5]), pack2(p[6], p[7])};
      const bf16x8 pf = __builtin_bit_cast(bf16x8, pw);
#pragma unroll
      for (int db = 0; db < 8; ++db) {
        bf16x8 vf;
#pragma unroll
        for (int j = 0; j < 8; ++j) vf[j] = (short)V[(32 * ks + 16 * (j >> 2) + 4 * g + (j & 3)) * 128 + 16 * db + i];
        o[db] = __builtin_amdgcn_mfma_f32_16x16x32_bf16(vf, pf, o[db], 0, 0, 0);
      }
    }
    lsum += __shfl_xor(lsum, 16); lsum += __shfl_xor(lsum, 32);
    const float r = 1.0f / lsum;
#pragma unroll
    for (int db = 0; db < 8; ++db) {
      uint2 w; w.x = pack2(o[db][0] * r, o[db][1] * r); w.y = pack2(o[db][2] * r, o[db][3] * r);
      *reinterpret_cast<uint2*>(O + row * 128 + 16 * db + 4 * g) = w;
    }
  }
}

__global__ __launch_bounds__(256, 1) void gqa_fwd(const unsigned short* __restrict__ q, const unsigned short* __restrict__ k,
                                                  const unsigned short* __restrict__ v, unsigned short* __restrict__ o) {
  const unsigned bid = blockIdx.x;
  const unsigned wv = __builtin_amdgcn_readfirstlane(threadIdx.x >> 6);
  unsigned flags;
  const unsigned short *qp, *kp, *vp; unsigned short* op;
  const auto ka = __builtin_amdgcn_kernarg_segment_ptr();
  asm volatile(
    "s_load_dwordx8 s[72:79], %[ka], 0x0\n\t"
    "v_mbcnt_lo_u32_b32 v16, -1, 0\n\t"
    "v_mbcnt_hi_u32_b32 v16, -1, v16\n\t"
    "s_mov_b32 s56, %[wv]\n\t"
    "s_getpc_b64 s[86:87]\n\t"
    "Lpc:\n\t"
    "s_lshl_b32 s69, s56, 13\n\t"
    "v_lshlrev_b32 v28, 7, v16\n\t"
    "v_add_u32 v28, s69, v28\n\t"
    "v_min_u32 v28, Lend-Lpc-4, v28\n\t"
    "global_load_dword v26, v28, s[86:87]\n\t"
    "v_lshrrev_b32 v17, 4, v16\n\t"
    "v_and_b32 v18, 15, v16\n\t"
    "v_sub_u32 v19, 0, v17\n\t"
    "v_and_b32 v19, 3, v19\n\t"
    "v_and_b32 v20, 3, v16\n\t"
    "v_xor_b32 v19, v19, v20\n\t"
    "v_lshlrev_b32 v19, 4, v19\n\t"
    "v_lshrrev_b32 v21, 2, v16\n\t"
    "v_lshl_add_u32 v4, v21, 8, v19\n\t"
    "s_and_b32 s63, %[bid], 7\n\t"
    "s_lshr_b32 s64, %[bid], 3\n\t"
    "s_lshl_b32 s63, s63, 4\n\t"
    "s_lshr_b32 s57, s64, 1\n\t"
    "s_add_u32 s63, s63, s57\n\t"
    "s_and_b32 s64, s64, 1\n\t"
    "s_lshl_b32 s58, s56, 12\n\t"
    "s_mov_b32 s59, s58\n\t"
    "s_mul_i32 s60, s56, 24576\n\t"
    "s_add_u32 s61, s60, 65536\n\t"
    "s_lshl_b32 s57, s63, 17\n\t"
    "s_mul_i32 s63, s63, 393216\n\t"
    "s_mul_i32 s64, s64, 196608\n\t"
    "s_add_u32 s63, s63, s64\n\t"
    "s_mov_b32 s38, 131072\n\t"
    "s_mov_b32 s39, 0x20000\n\t"
    "s_mov_b32 s42, 131072\n\t"
    "s_mov_b32 s43, 0x20000\n\t"
    "s_mov_b32 s46, 196608\n\t"
    "s_mov_b32 s47, 0x20000\n\t"
    "s_mov_b32 s50, 196608\n\t"
    "s_mov_b32 s51, 0x20000\n\t"
    "s_waitcnt lgkmcnt(0)\n\t"
    "s_add_u32 s36, s74, s57\n\t"
    "s_addc_u32 s37, s75, 0\n\t"
    "s_and_b32 s37, s37, 0xffff\n\t"
    "s_add_u32 s44, s72, s63\n\t"
    "s_addc_u32 s45, s73, 0\n\t"
    "s_and_b32 s45, s45, 0xffff\n\t"
    "s_add_u32 s40, s76, s57\n\t"
    "s_addc_u32 s41, s77, 0\n\t"
    "s_and_b32 s41, s41, 0xffff\n\t"
    "s_add_u32 s48, s78, s63\n\t"
    "s_addc_u32 s49, s79, 0\n\t"
    "s_and_b32 s49, s49, 0xffff\n\t"
    "s_mov_b64 %[qp], s[72:73]\n\t"
    "s_mov_b64 %[kp], s[74:75]\n\t"
    "s_mov_b64 %[vp], s[76:77]\n\t"
    "s_mov_b64 %[op], s[78:79]\n\t"
    "s_add_u32 m0, s59, 0\n\t"
    "s_add_u32 s63, s58, 0\n\t"
    "buffer_load_dwordx4 v4, s[36:39], s63 offen offset:0 lds\n\t"
    "s_add_u32 m0, s59, 960\n\t"
    "s_add_u32 s63, s58, 0\n\t"
    "buffer_load_dwordx4 v4, s[36:39], s63 offen offset:64 lds\n\t"
    "s_add_u32 m0, s59, 1920\n\t"
    "s_add_u32 s63, s58, 0\n\t"
    "buffer_load_dwordx4 v4, s[36:39], s63 offen offset:128 lds\n\t"
    "s_add_u32 m0, s59, 2880\n\t"
    "s_add_u32 s63, s58, 0\n\t"
    "buffer_load_dwordx4 v4, s[36:39], s63 offen offset:192 lds\n\t"
    "s_add_u32 s63, s60, 0\n\t"
    "s_add_u32 m0, s61, 0\n\t"
    "s_nop 0\n\t"
    "buffer_load_dwordx4 v4, s[44:47], s63 offen offset:0 lds\n\t"
    "s_add_u32 m0, s61, 960\n\t"
    "s_nop 0\n\t"
    "buffer_load_dwordx4 v4, s[44:47], s63 offen offset:64 lds\n\t"
    "s_add_u32 m0, s61, 1920\n\t"
    "s_nop 0\n\t"
    "buffer_load_dwordx4 v4, s[44:47], s63 offen offset:128 lds\n\t"
    "s_add_u32 m0, s61, 2880\n\t"
    "s_nop 0\n\t"
    "buffer_load_dwordx4 v4, s[44:47], s63 offen offset:192 lds\n\t"
    "v_lshrrev_b32 v21, 2, v16\n\t"
    "v_and_b32 v21, 7, v21\n\t"
    "v_lshlrev_b32 v21, 8, v21\n\t"
    "v_and_b32 v22, 1, v17\n\t"
    "v_lshlrev_b32 v22, 1, v22\n\t"
    "v_xor_b32 v22, v22, v20\n\t"
    "v_lshl_add_u32 v21, v22, 4, v21\n\t"
    "v_lshrrev_b32 v22, 5, v16\n\t"
    "v_lshl_add_u32 v5, v22, 6, v21\n\t"
    "v_lshrrev_b32 v19, 2, v18\n\t"
    "v_sub_u32 v19, 0, v19\n\t"
    "v_and_b32 v19, 3, v19\n\t"
    "v_xor_b32 v19, v19, v17\n\t"
    "v_lshlrev_b32 v19, 4, v19\n\t"
    "v_lshl_add_u32 v0, v18, 6, v19\n\t"
    "v_add_u32 v6, s61, v0\n\t"
    "v_lshrrev_b32 v19, 1, v17\n\t"
    "v_lshlrev_b32 v19, 11, v19\n\t"
    "v_and_b32 v20, 1, v17\n\t"
    "v_lshlrev_b32 v21, 8, v20\n\t"
    "v_add_u32 v19, v19, v21\n\t"
    "v_lshlrev_b32 v21, 5, v20\n\t"
    "v_add_u32 v19, v19, v21\n\t"
    "v_lshrrev_b32 v21, 2, v16\n\t"
    "v_and_b32 v21, 3, v21\n\t"
    "v_lshl_add_u32 v19, v21, 6, v19\n\t"
    "v_and_b32 v21, 3, v16\n\t"
    "v_lshl_add_u32 v19, v21, 3, v19\n\t"
    "v_add_u32 v1, 0x8000, v19\n\t"
    "v_xor_b32 v2, 32, v1\n\t"
    "v_mul_u32_u24 v19, 272, v18\n\t"
    "v_lshl_add_u32 v19, v17, 3, v19\n\t"
    "v_add_u32 v7, s61, v19\n\t"
    "v_mul_u32_u24 v19, 272, v17\n\t"
    "v_lshl_add_u32 v19, v18, 4, v19\n\t"
    "v_add_u32 v8, s61, v19\n\t"
    "v_lshlrev_b32 v9, 4, v16\n\t"
    "v_and_b32 v19, 1, v18\n\t"
    "v_mul_u32_u24 v19, 0xAA, v19\n\t"
    "v_add_u32 v19, 0x44, v19\n\t"
    "v_lshl_or_b32 v19, v19, 8, v19\n\t"
    "v_lshl_or_b32 v3, v19, 16, v19\n\t"
    "v_mov_b32 v10, 0\n\t"
    "v_mov_b32 v19, 0x3f803f80\n\t"
    "v_accvgpr_write_b32 a248, v19\n\t"
    "v_accvgpr_write_b32 a249, v19\n\t"
    "v_accvgpr_write_b32 a250, v19\n\t"
    "v_accvgpr_write_b32 a251, v19\n\t"
    "v_accvgpr_write_b32 a224, 0\n\t"
    "v_accvgpr_write_b32 a225, 0\n\t"
    "v_accvgpr_write_b32 a226, 0\n\t"
    "v_accvgpr_write_b32 a227, 0\n\t"
    "v_accvgpr_write_b32 a228, 0\n\t"
    "v_accvgpr_write_b32 a229, 0\n\t"
    "v_accvgpr_write_b32 a230, 0\n\t"
    "v_accvgpr_write_b32 a231, 0\n\t"
    "v_accvgpr_write_b32 a232, 0\n\t"
    "v_accvgpr_write_b32 a233, 0\n\t"
    "v_accvgpr_write_b32 a234, 0\n\t"
    "v_accvgpr_write_b32 a235, 0\n\t"
    "v_accvgpr_write_b32 a236, 0\n\t"
    "v_accvgpr_write_b32 a237, 0\n\t"
    "v_accvgpr_write_b32 a238, 0\n\t"
    "v_accvgpr_write_b32 a239, 0\n\t"
    "v_accvgpr_write_b32 a240, 0\n\t"
    "v_accvgpr_write_b32 a241, 0\n\t"
    "v_accvgpr_write_b32 a242, 0\n\t"
    "v_accvgpr_write_b32 a243, 0\n\t"
    "v_accvgpr_write_b32 a244, 0\n\t"
    "v_accvgpr_write_b32 a245, 0\n\t"
    "v_accvgpr_write_b32 a246, 0\n\t"
    "v_accvgpr_write_b32 a247, 0\n\t"
    "s_mov_b32 s52, 0x3e0293ee\n\t"
    "s_mov_b32 s53, 0xffff0000\n\t"
    "s_mov_b32 s54, 0x30800000\n\t"
    "s_mov_b32 s55, 0x1e000000\n\t"
    "s_mov_b32 s67, 196608\n\t"
    "s_add_u32 s63, s60, 4096\n\t"
    "s_add_u32 m0, s61, 4096\n\t"
    "s_nop 0\n\t"
    "buffer_load_dwordx4 v4, s[44:47], s63 offen offset:0 lds\n\t"
    "s_add_u32 m0, s61, 5056\n\t"
    "s_nop 0\n\t"
    "buffer_load_dwordx4 v4, s[44:47], s63 offen offset:64 lds\n\t"
    "s_add_u32 m0, s61, 6016\n\t"
    "s_nop 0\n\t"
    "buffer_load_dwordx4 v4, s[44:47], s63 offen offset:128 lds\n\t"
    "s_add_u32 m0, s61, 6976\n\t"
    "s_nop 0\n\t"
    "buffer_load_dwordx4 v4, s[44:47], s63 offen offset:192 lds\n\t"
    "s_add_u32 s68, s58, 16384\n\t"
    "s_waitcnt vmcnt(8)\n\t"
    "s_barrier\n\t"
    "ds_read_b128 a[96:99], v0 offset:0\n\t"
    "ds_read_b128 a[100:103], v0 offset:1024\n\t"
    "ds_read_b128 a[104:107], v0 offset:2048\n\t"
    "ds_read_b128 a[108:111], v0 offset:3072\n\t"
    "ds_read_b128 a[112:115], v0 offset:4096\n\t"
    "ds_read_b128 a[116:119], v0 offset:5120\n\t"
    "ds_read_b128 a[120:123], v0 offset:6144\n\t"
    "ds_read_b128 a[124:127], v0 offset:7168\n\t"
    "ds_read_b128 a[128:131], v0 offset:8192\n\t"
    "ds_read_b128 a[132:135], v0 offset:9216\n\t"
    "ds_read_b128 a[136:139], v0 offset:10240\n\t"
    "ds_read_b128 a[140:143], v0 offset:11264\n\t"
    "ds_read_b128 a[144:147], v0 offset:12288\n\t"
    "ds_read_b128 a[148:151], v0 offset:13312\n\t"
    "ds_read_b128 a[152:155], v0 offset:14336\n\t"
    "ds_read_b128 a[156:159], v0 offset:15360\n\t"
    "v_xor_b32 v0, 0x4000, v0\n\t"
    "s_waitcnt vmcnt(4)\n\t"
    "s_add_u32 s63, s58, 0\n\t"
    "s_add_u32 m0, s59, 32768\n\t"
    "s_nop 0\n\t"
    "buffer_load_dwordx4 v5, s[40:43], s63 offen offset:0 lds\n\t"
    "buffer_load_dwordx4 v5, s[40:43], s63 offen offset:2048 lds\n\t"
    "s_add_u32 m0, s59, 33664\n\t"
    "s_nop 0\n\t"
    "buffer_load_dwordx4 v5, s[40:43], s63 offen offset:128 lds\n\t"
    "buffer_load_dwordx4 v5, s[40:43], s63 offen offset:2176 lds\n\t"
    "ds_read_b128 v[224:227], v6 offset:0\n\t"
    "ds_read_b128 v[228:231], v6 offset:1024\n\t"
    "ds_read_b128 v[232:235], v6 offset:2048\n\t"
    "ds_read_b128 v[236:239], v6 offset:3072\n\t"
    "s_waitcnt lgkmcnt(3)\n\t"
    "v_lshlrev_b32 v24, 16, v224\n\t"
    "v_and_b32 v224, s53, v224\n\t"
    "v_mul_f32 v24, s52, v24\n\t"
    "v_mul_f32 v224, s52, v224\n\t"
    "v_cvt_pk_bf16_f32 v224, v24, v224\n\t"
    "v_accvgpr_write_b32 a0, v224\n\t"
    "v_lshlrev_b32 v25, 16, v225\n\t"
    "v_and_b32 v225, s53, v225\n\t"
    "v_mul_f32 v25, s52, v25\n\t"
    "v_mul_f32 v225, s52, v225\n\t"
    "v_cvt_pk_bf16_f32 v225, v25, v225\n\t"
    "v_accvgpr_write_b32 a1, v225\n\t"
    "v_lshlrev_b32 v24, 16, v226\n\t"
    "v_and_b32 v226, s53, v226\n\t"
    "v_mul_f32 v24, s52, v24\n\t"
    "v_mul_f32 v226, s52, v226\n\t"
    "v_cvt_pk_bf16_f32 v226, v24, v226\n\t"
    "v_accvgpr_write_b32 a2, v226\n\t"
    "v_lshlrev_b32 v25, 16, v227\n\t"
    "v_and_b32 v227, s53, v227\n\t"
    "v_mul_f32 v25, s52, v25\n\t"
    "v_mul_f32 v227, s52, v227\n\t"
    "v_cvt_pk_bf16_f32 v227, v25, v227\n\t"
    "v_accvgpr_write_b32 a3, v227\n\t"
    "s_waitcnt lgkmcnt(2)\n\t"
    "v_lshlrev_b32 v24, 16, v228\n\t"
    "v_and_b32 v228, s53, v228\n\t"
    "v_mul_f32 v24, s52, v24\n\t"
    "v_mul_f32 v228, s52, v228\n\t"
    "v_cvt_pk_bf16_f32 v228, v24, v228\n\t"
    "v_accvgpr_write_b32 a4, v228\n\t"
    "v_lshlrev_b32 v25, 16, v229\n\t"
    "v_and_b32 v229, s53, v229\n\t"
    "v_mul_f32 v25, s52, v25\n\t"
    "v_mul_f32 v229, s52, v229\n\t"
    "v_cvt_pk_bf16_f32 v229, v25, v229\n\t"
    "v_accvgpr_write_b32 a5, v229\n\t"
    "v_lshlrev_b32 v24, 16, v230\n\t"
    "v_and_b32 v230, s53, v230\n\t"
    "v_mul_f32 v24, s52, v24\n\t"
    "v_mul_f32 v230, s52, v230\n\t"
    "v_cvt_pk_bf16_f32 v230, v24, v230\n\t"
    "v_accvgpr_write_b32 a6, v230\n\t"
    "v_lshlrev_b32 v25, 16, v231\n\t"
    "v_and_b32 v231, s53, v231\n\t"
    "v_mul_f32 v25, s52, v25\n\t"
    "v_mul_f32 v231, s52, v231\n\t"
    "v_cvt_pk_bf16_f32 v231, v25, v231\n\t"
    "v_accvgpr_write_b32 a7, v231\n\t"
    "s_waitcnt lgkmcnt(1)\n\t"
    "v_lshlrev_b32 v24, 16, v232\n\t"
    "v_and_b32 v232, s53, v232\n\t"
    "v_mul_f32 v24, s52, v24\n\t"
    "v_mul_f32 v232, s52, v232\n\t"
    "v_cvt_pk_bf16_f32 v232, v24, v232\n\t"
    "v_accvgpr_write_b32 a8, v232\n\t"
    "v_lshlrev_b32 v25, 16, v233\n\t"
    "v_and_b32 v233, s53, v233\n\t"
    "v_mul_f32 v25, s52, v25\n\t"
    "v_mul_f32 v233, s52, v233\n\t"
    "v_cvt_pk_bf16_f32 v233, v25, v233\n\t"
    "v_accvgpr_write_b32 a9, v233\n\t"
    "v_lshlrev_b32 v24, 16, v234\n\t"
    "v_and_b32 v234, s53, v234\n\t"
    "v_mul_f32 v24, s52, v24\n\t"
    "v_mul_f32 v234, s52, v234\n\t"
    "v_cvt_pk_bf16_f32 v234, v24, v234\n\t"
    "v_accvgpr_write_b32 a10, v234\n\t"
    "v_lshlrev_b32 v25, 16, v235\n\t"
    "v_and_b32 v235, s53, v235\n\t"
    "v_mul_f32 v25, s52, v25\n\t"
    "v_mul_f32 v235, s52, v235\n\t"
    "v_cvt_pk_bf16_f32 v235, v25, v235\n\t"
    "v_accvgpr_write_b32 a11, v235\n\t"
    "s_waitcnt lgkmcnt(0)\n\t"
    "v_lshlrev_b32 v24, 16, v236\n\t"
    "v_and_b32 v236, s53, v236\n\t"
    "v_mul_f32 v24, s52, v24\n\t"
    "v_mul_f32 v236, s52, v236\n\t"
    "v_cvt_pk_bf16_f32 v236, v24, v236\n\t"
    "v_accvgpr_write_b32 a12, v236\n\t"
    "v_lshlrev_b32 v25, 16, v237\n\t"
    "v_and_b32 v237, s53, v237\n\t"
    "v_mul_f32 v25, s52, v25\n\t"
    "v_mul_f32 v237, s52, v237\n\t"
    "v_cvt_pk_bf16_f32 v237, v25, v237\n\t"
    "v_accvgpr_write_b32 a13, v237\n\t"
    "v_lshlrev_b32 v24, 16, v238\n\t"
    "v_and_b32 v238, s53, v238\n\t"
    "v_mul_f32 v24, s52, v24\n\t"
    "v_mul_f32 v238, s52, v238\n\t"
    "v_cvt_pk_bf16_f32 v238, v24, v238\n\t"
    "v_accvgpr_write_b32 a14, v238\n\t"
    "v_lshlrev_b32 v25, 16, v239\n\t"
    "v_and_b32 v239, s53, v239\n\t"
    "v_mul_f32 v25, s52, v25\n\t"
    "v_mul_f32 v239, s52, v239\n\t"
    "v_cvt_pk_bf16_f32 v239, v25, v239\n\t"
    "v_accvgpr_write_b32 a15, v239\n\t"
    "v_mfma_f32_16x16x32_bf16 v[48:51], a[96:99], a[0:3], 0\n\t"
    "s_waitcnt vmcnt(4)\n\t"
    "s_add_u32 s69, s60, 8192\n\t"
    "s_add_u32 m0, s61, 8192\n\t"
    "ds_read_b128 v[240:243], v6 offset:4096\n\t"
    "ds_read_b128 v[244:247], v6 offset:5120\n\t"
    "v_mfma_f32_16x16x32_bf16 v[48:51], a[100:103], a[4:7], v[48:51]\n\t"
    "ds_read_b128 v[248:251], v6 offset:6144\n\t"
    "ds_read_b128 v[252:255], v6 offset:7168\n\t"
    "v_mfma_f32_16x16x32_bf16 v[48:51], a[104:107], a[8:11], v[48:51]\n\t"
    "buffer_load_dwordx4 v4, s[44:47], s69 offen offset:0 lds\n\t"
    "v_mfma_f32_16x16x32_bf16 v[48:51], a[108:111], a[12:15], v[48:51]\n\t"
    "v_mfma_f32_16x16x32_bf16 v[52:55], a[112:115], a[0:3], 0\n\t"
    "v_mfma_f32_16x16x32_bf16 v[52:55], a[116:119], a[4:7], v[52:55]\n\t"
    "v_mfma_f32_16x16x32_bf16 v[52:55], a[120:123], a[8:11], v[52:55]\n\t"
    "s_add_u32 m0, s61, 9152\n\t"
    "s_waitcnt lgkmcnt(3)\n\t"
    "v_lshlrev_b32 v24, 16, v240\n\t"
    "v_and_b32 v240, s53, v240\n\t"
    "v_mfma_f32_16x16x32_bf16 v[52:55], a[124:127], a[12:15], v[52:55]\n\t"
    "buffer_load_dwordx4 v4, s[44:47], s69 offen offset:64 lds\n\t"
    "v_mul_f32 v24, s52, v24\n\t"
    "v_mfma_f32_16x16x32_bf16 v[56:59], a[128:131], a[0:3], 0\n\t"
    "v_mul_f32 v240, s52, v240\n\t"
    "v_cvt_pk_bf16_f32 v240, v24, v240\n\t"
    "v_mfma_f32_16x16x32_bf16 v[56:59], a[132:135], a[4:7], v[56:59]\n\t"
    "v_accvgpr_write_b32 a16, v240\n\t"
    "v_lshlrev_b32 v25, 16, v241\n\t"
    "v_mfma_f32_16x16x32_bf16 v[56:59], a[136:139], a[8:11], v[56:59]\n\t"
    "v_and_b32 v241, s53, v241\n\t"
    "v_mul_f32 v25, s52, v25\n\t"
    "v_mfma_f32_16x16x32_bf16 v[56:59], a[140:143], a[12:15], v[56:59]\n\t"
    "v_mul_f32 v241, s52, v241\n\t"
    "v_cvt_pk_bf16_f32 v241, v25, v241\n\t"
    "v_mfma_f32_16x16x32_bf16 v[60:63], a[144:147], a[0:3], 0\n\t"
    "v_accvgpr_write_b32 a17, v241\n\t"
    "v_lshlrev_b32 v24, 16, v242\n\t"
    "v_mfma_f32_16x16x32_bf16 v[60:63], a[148:151], a[4:7], v[60:63]\n\t"
    "s_add_u32 m0, s61, 10112\n\t"
    "v_and_b32 v242, s53, v242\n\t"
    "v_mul_f32 v24, s52, v24\n\t"
    "v_mul_f32 v242, s52, v242\n\t"
    "v_cvt_pk_bf16_f32 v242, v24, v242\n\t"
    "v_accvgpr_write_b32 a18, v242\n\t"
    "v_lshlrev_b32 v25, 16, v243\n\t"
    "v_and_b32 v243, s53, v243\n\t"
    "v_mul_f32 v25, s52, v25\n\t"
    "v_mul_f32 v243, s52, v243\n\t"
    "v_cvt_pk_bf16_f32 v243, v25, v243\n\t"
    "v_accvgpr_write_b32 a19, v243\n\t"
    "s_waitcnt lgkmcnt(2)\n\t"
    "v_lshlrev_b32 v24, 16, v244\n\t"
    "v_and_b32 v244, s53, v244\n\t"
    "v_mul_f32 v24, s52, v24\n\t"
    "v_mul_f32 v244, s52, v244\n\t"
    "v_cvt_pk_bf16_f32 v244, v24, v244\n\t"
    "v_accvgpr_write_b32 a20, v244\n\t"
    "v_lshlrev_b32 v25, 16, v245\n\t"
    "v_and_b32 v245, s53, v245\n\t"
    "v_mul_f32 v25, s52, v25\n\t"
    "v_mul_f32 v245, s52, v245\n\t"
    "v_cvt_pk_bf16_f32 v245, v25, v245\n\t"
    "v_accvgpr_write_b32 a21, v245\n\t"
    "v_lshlrev_b32 v24, 16, v246\n\t"
    "v_and_b32 v246, s53, v246\n\t"
    "v_mul_f32 v24, s52, v24\n\t"
    "v_mul_f32 v246, s52, v246\n\t"
    "v_cvt_pk_bf16_f32 v246, v24, v246\n\t"
    "v_accvgpr_write_b32 a22, v246\n\t"
    "v_lshlrev_b32 v25, 16, v247\n\t"
    "v_and_b32 v247, s53, v247\n\t"
    "v_mul_f32 v25, s52, v25\n\t"
    "v_mul_f32 v247, s52, v247\n\t"
    "v_cvt_pk_bf16_f32 v247, v25, v247\n\t"
    "v_accvgpr_write_b32 a23, v247\n\t"
    "s_waitcnt lgkmcnt(1)\n\t"
    "v_lshlrev_b32 v24, 16, v248\n\t"
    "v_and_b32 v248, s53, v248\n\t"
    "v_mul_f32 v24, s52, v24\n\t"
    "v_mul_f32 v248, s52, v248\n\t"
    "v_cvt_pk_bf16_f32 v248, v24, v248\n\t"
    "v_accvgpr_write_b32 a24, v248\n\t"
    "v_lshlrev_b32 v25, 16, v249\n\t"
    "v_and_b32 v249, s53, v249\n\t"
    "v_mul_f32 v25, s52, v25\n\t"
    "v_mul_f32 v249, s52, v249\n\t"
    "v_cvt_pk_bf16_f32 v249, v25, v249\n\t"
    "v_accvgpr_write_b32 a25, v249\n\t"
    "v_lshlrev_b32 v24, 16, v250\n\t"
    "v_and_b32 v250, s53, v250\n\t"
    "v_mul_f32 v24, s52, v24\n\t"
    "v_mul_f32 v250, s52, v250\n\t"
    "v_cvt_pk_bf16_f32 v250, v24, v250\n\t"
    "v_accvgpr_write_b32 a26, v250\n\t"
    "v_lshlrev_b32 v25, 16, v251\n\t"
    "v_and_b32 v251, s53, v251\n\t"
    "v_mul_f32 v25, s52, v25\n\t"
    "v_mul_f32 v251, s52, v251\n\t"
    "v_cvt_pk_bf16_f32 v251, v25, v251\n\t"
    "v_accvgpr_write_b32 a27, v251\n\t"
    "s_waitcnt lgkmcnt(0)\n\t"
    "v_lshlrev_b32 v24, 16, v252\n\t"
    "v_and_b32 v252, s53, v252\n\t"
    "v_mul_f32 v24, s52, v24\n\t"
    "v_mul_f32 v252, s52, v252\n\t"
    "v_cvt_pk_bf16_f32 v252, v24, v252\n\t"
    "v_accvgpr_write_b32 a28, v252\n\t"
    "v_lshlrev_b32 v25, 16, v253\n\t"
    "v_and_b32 v253, s53, v253\n\t"
    "v_mul_f32 v25, s52, v25\n\t"
    "v_mul_f32 v253, s52, v253\n\t"
    "v_cvt_pk_bf16_f32 v253, v25, v253\n\t"
    "v_accvgpr_write_b32 a29, v253\n\t"
    "v_lshlrev_b32 v24, 16, v254\n\t"
    "v_and_b32 v254, s53, v254\n\t"
    "v_mul_f32 v24, s52, v24\n\t"
    "v_mul_f32 v254, s52, v254\n\t"
    "v_cvt_pk_bf16_f32 v254, v24, v254\n\t"
    "v_accvgpr_write_b32 a30, v254\n\t"
    "v_lshlrev_b32 v25, 16, v255\n\t"
    "v_and_b32 v255, s53, v255\n\t"
    "v_mul_f32 v25, s52, v25\n\t"
    "v_mul_f32 v255, s52, v255\n\t"
    "v_cvt_pk_bf16_f32 v255, v25, v255\n\t"
    "v_accvgpr_write_b32 a31, v255\n\t"
    "v_mfma_f32_16x16x32_bf16 v[60:63], a[152:155], a[8:11], v[60:63]\n\t"
    "buffer_load_dwordx4 v4, s[44:47], s69 offen offset:128 lds\n\t"
    "v_exp_f32 v48, v48\n\t"
    "v_exp_f32 v49, v49\n\t"
    "v_exp_f32 v50, v50\n\t"
    "v_exp_f32 v51, v51\n\t"
    "v_mfma_f32_16x16x32_bf16 v[60:63], a[156:159], a[12:15], v[60:63]\n\t"
    "v_cvt_pk_bf16_f32 v32, v48, v49\n\t"
    "v_cvt_pk_bf16_f32 v33, v50, v51\n\t"
    "v_mfma_f32_16x16x32_bf16 v[48:51], a[96:99], a[16:19], 0\n\t"
    "s_waitcnt vmcnt(3)\n\t"
    "v_exp_f32 v52, v52\n\t"
    "v_mfma_f32_16x16x32_bf16 v[48:51], a[100:103], a[20:23], v[48:51]\n\t"
    "s_barrier\n\t"
    "v_exp_f32 v53, v53\n\t"
    "v_mfma_f32_16x16x32_bf16 v[48:51], a[104:107], a[24:27], v[48:51]\n\t"
    "v_exp_f32 v54, v54\n\t"
    "v_exp_f32 v55, v55\n\t"
    "v_mfma_f32_16x16x32_bf16 v[48:51], a[108:111], a[28:31], v[48:51]\n\t"
    "ds_read_b64_tr_b16 a[160:161], v1 offset:0\n\t"
    "v_cvt_pk_bf16_f32 v34, v52, v53\n\t"
    "v_cvt_pk_bf16_f32 v35, v54, v55\n\t"
    "v_mfma_f32_16x16x32_bf16 v[52:55], a[112:115], a[16:19], 0\n\t"
    "s_add_u32 m0, s61, 11072\n\t"
    "v_exp_f32 v56, v56\n\t"
    "v_mfma_f32_16x16x32_bf16 v[52:55], a[116:119], a[20:23], v[52:55]\n\t"
    "buffer_load_dwordx4 v4, s[44:47], s69 offen offset:192 lds\n\t"
    "s_add_u32 s69, s60, 12288\n\t"
    "s_add_u32 m0, s61, 12288\n\t"
    "s_waitcnt vmcnt(0)\n\t"
    "ds_read_b128 v[224:227], v6 offset:8192\n\t"
    "v_mfma_f32_16x16x32_bf16 v[52:55], a[120:123], a[24:27], v[52:55]\n\t"
    "buffer_load_dwordx4 v4, s[44:47], s69 offen offset:0 lds\n\t"
    "s_add_u32 m0, s61, 13248\n\t"
    "ds_read_b128 v[228:231], v6 offset:9216\n\t"
    "v_exp_f32 v57, v57\n\t"
    "v_exp_f32 v58, v58\n\t"
    "v_exp_f32 v59, v59\n\t"
    "v_mfma_f32_16x16x32_bf16 v[52:55], a[124:127], a[28:31], v[52:55]\n\t"
    "buffer_load_dwordx4 v4, s[44:47], s69 offen offset:64 lds\n\t"
    "s_add_u32 m0, s61, 14208\n\t"
    "ds_read_b128 v[232:235], v6 offset:10240\n\t"
    "v_cvt_pk_bf16_f32 v36, v56, v57\n\t"
    "v_cvt_pk_bf16_f32 v37, v58, v59\n\t"
    "v_mfma_f32_16x16x32_bf16 v[56:59], a[128:131], a[16:19], 0\n\t"
    "buffer_load_dwordx4 v4, s[44:47], s69 offen offset:128 lds\n\t"
    "s_add_u32 m0, s61, 15168\n\t"
    "ds_read_b128 v[236:239], v6 offset:11264\n\t"
    "v_mfma_f32_16x16x32_bf16 v[56:59], a[132:135], a[20:23], v[56:59]\n\t"
    "buffer_load_dwordx4 v4, s[44:47], s69 offen offset:192 lds\n\t"
    "s_add_u32 s69, s60, 16384\n\t"
    "s_add_u32 m0, s61, 16384\n\t"
    "ds_read_b64_tr_b16 a[162:163], v1 offset:4096\n\t"
    "v_mfma_f32_16x16x32_bf16 v[56:59], a[136:139], a[24:27], v[56:59]\n\t"
    "buffer_load_dwordx4 v4, s[44:47], s69 offen offset:0 lds\n\t"
    "s_add_u32 m0, s61, 17344\n\t"
    "v_exp_f32 v60, v60\n\t"
    "v_exp_f32 v61, v61\n\t"
    "v_exp_f32 v62, v62\n\t"
    "v_exp_f32 v63, v63\n\t"
    "v_mfma_f32_16x16x32_bf16 v[56:59], a[140:143], a[28:31], v[56:59]\n\t"
    "buffer_load_dwordx4 v4, s[44:47], s69 offen offset:64 lds\n\t"
    "s_add_u32 m0, s61, 18304\n\t"
    "s_waitcnt lgkmcnt(4)\n\t"
    "v_lshlrev_b32 v24, 16, v224\n\t"
    "ds_read_b64_tr_b16 a[164:165], v1 offset:8192\n\t"
    "v_cvt_pk_bf16_f32 v38, v60, v61\n\t"
    "ds_read_b64_tr_b16 a[166:167], v1 offset:12288\n\t"
    "v_cvt_pk_bf16_f32 v39, v62, v63\n\t"
    "v_mfma_f32_16x16x32_bf16 v[60:63], a[144:147], a[16:19], 0\n\t"
    "buffer_load_dwordx4 v4, s[44:47], s69 offen offset:128 lds\n\t"
    "s_add_u32 m0, s61, 19264\n\t"
    "v_and_b32 v224, s53, v224\n\t"
    "ds_read_b64_tr_b16 a[168:169], v2 offset:0\n\t"
    "ds_read_b64_tr_b16 a[170:171], v2 offset:4096\n\t"
    "v_mfma_f32_16x16x32_bf16 v[60:63], a[148:151], a[20:23], v[60:63]\n\t"
    "buffer_load_dwordx4 v4, s[44:47], s69 offen offset:192 lds\n\t"
    "s_add_u32 s69, s60, 20480\n\t"
    "s_add_u32 m0, s61, 20480\n\t"
    "v_mul_f32 v24, s52, v24\n\t"
    "ds_read_b64_tr_b16 a[172:173], v2 offset:8192\n\t"
    "ds_read_b64_tr_b16 a[174:175], v2 offset:12288\n\t"
    "v_mfma_f32_16x16x32_bf16 v[60:63], a[152:155], a[24:27], v[60:63]\n\t"
    "buffer_load_dwordx4 v4, s[44:47], s69 offen offset:0 lds\n\t"
    "s_add_u32 m0, s61, 21440\n\t"
    "v_mul_f32 v224, s52, v224\n\t"
    "ds_read_b64_tr_b16 a[176:177], v1 offset:512\n\t"
    "ds_read_b64_tr_b16 a[178:179], v1 offset:4608\n\t"
    "v_mfma_f32_16x16x32_bf16 v[60:63], a[156:159], a[28:31], v[60:63]\n\t"
    "buffer_load_dwordx4 v4, s[44:47], s69 offen offset:64 lds\n\t"
    "s_add_u32 m0, s61, 22400\n\t"
    "v_cvt_pk_bf16_f32 v224, v24, v224\n\t"
    "ds_read_b64_tr_b16 a[180:181], v1 offset:8704\n\t"
    "ds_read_b64_tr_b16 a[182:183], v1 offset:12800\n\t"
    "v_smfmac_f32_16x16x64_bf16 a[224:227], a[248:251], v[32:39], v3\n\t"
    "s_waitcnt lgkmcnt(10)\n\t"
    "buffer_load_dwordx4 v4, s[44:47], s69 offen offset:128 lds\n\t"
    "s_add_u32 m0, s61, 23360\n\t"
    "v_accvgpr_write_b32 a32, v224\n\t"
    "ds_read_b64_tr_b16 a[184:185], v2 offset:512\n\t"
    "ds_read_b64_tr_b16 a[186:187], v2 offset:4608\n\t"
    "v_mfma_f32_16x16x32_bf16 v[64:67], a[160:163], v[32:35], 0\n\t"
    "s_waitcnt lgkmcnt(10)\n\t"
    "buffer_load_dwordx4 v4, s[44:47], s69 offen offset:192 lds\n\t"
    "v_lshlrev_b32 v25, 16, v225\n\t"
    "ds_read_b64_tr_b16 a[188:189], v2 offset:8704\n\t"
    "ds_read_b64_tr_b16 a[190:191], v2 offset:12800\n\t"
    "v_mfma_f32_16x16x32_bf16 v[64:67], a[164:167], v[36:39], v[64:67]\n\t"
    "s_waitcnt lgkmcnt(10)\n\t"
    "ds_read_b64_tr_b16 a[192:193], v1 offset:1024\n\t"
    "v_and_b32 v225, s53, v225\n\t"
    "ds_read_b64_tr_b16 a[194:195], v1 offset:5120\n\t"
    "v_mfma_f32_16x16x32_bf16 v[68:71], a[168:171], v[32:35], 0\n\t"
    "s_waitcnt lgkmcnt(10)\n\t"
    "ds_read_b64_tr_b16 a[196:197], v1 offset:9216\n\t"
    "v_mul_f32 v25, s52, v25\n\t"
    "ds_read_b64_tr_b16 a[198:199], v1 offset:13312\n\t"
    "v_mfma_f32_16x16x32_bf16 v[68:71], a[172:175], v[36:39], v[68:71]\n\t"
    "s_waitcnt lgkmcnt(10)\n\t"
    "ds_read_b64_tr_b16 a[200:201], v2 offset:1024\n\t"
    "v_mul_f32 v225, s52, v225\n\t"
    "ds_read_b64_tr_b16 a[202:203], v2 offset:5120\n\t"
    "v_mfma_f32_16x16x32_bf16 v[72:75], a[176:179], v[32:35], 0\n\t"
    "s_waitcnt lgkmcnt(10)\n\t"
    "ds_read_b64_tr_b16 a[204:205], v2 offset:9216\n\t"
    "v_cvt_pk_bf16_f32 v225, v25, v225\n\t"
    "ds_read_b64_tr_b16 a[206:207], v2 offset:13312\n\t"
    "v_mfma_f32_16x16x32_bf16 v[72:75], a[180:183], v[36:39], v[72:75]\n\t"
    "s_waitcnt lgkmcnt(10)\n\t"
    "ds_read_b64_tr_b16 a[208:209], v1 offset:1536\n\t"
    "v_accvgpr_write_b32 a33, v225\n\t"
    "ds_read_b64_tr_b16 a[210:211], v1 offset:5632\n\t"
    "v_mfma_f32_16x16x32_bf16 v[76:79], a[184:187], v[32:35], 0\n\t"
    "s_waitcnt lgkmcnt(10)\n\t"
    "ds_read_b64_tr_b16 a[212:213], v1 offset:9728\n\t"
    "v_lshlrev_b32 v24, 16, v226\n\t"
    "ds_read_b64_tr_b16 a[214:215], v1 offset:13824\n\t"
    "v_mfma_f32_16x16x32_bf16 v[76:79], a[188:191], v[36:39], v[76:79]\n\t"
    "s_waitcnt lgkmcnt(10)\n\t"
    "ds_read_b64_tr_b16 a[216:217], v2 offset:1536\n\t"
    "v_and_b32 v226, s53, v226\n\t"
    "ds_read_b64_tr_b16 a[218:219], v2 offset:5632\n\t"
    "v_mfma_f32_16x16x32_bf16 v[80:83], a[192:195], v[32:35], 0\n\t"
    "s_waitcnt lgkmcnt(10)\n\t"
    "ds_read_b64_tr_b16 a[220:221], v2 offset:9728\n\t"
    "v_mul_f32 v24, s52, v24\n\t"
    "ds_read_b64_tr_b16 a[222:223], v2 offset:13824\n\t"
    "v_mfma_f32_16x16x32_bf16 v[80:83], a[196:199], v[36:39], v[80:83]\n\t"
    "s_waitcnt lgkmcnt(10)\n\t"
    "v_mul_f32 v226, s52, v226\n\t"
    "v_cvt_pk_bf16_f32 v226, v24, v226\n\t"
    "v_mfma_f32_16x16x32_bf16 v[84:87], a[200:203], v[32:35], 0\n\t"
    "s_waitcnt lgkmcnt(8)\n\t"
    "v_accvgpr_write_b32 a34, v226\n\t"
    "v_lshlrev_b32 v25, 16, v227\n\t"
    "v_mfma_f32_16x16x32_bf16 v[84:87], a[204:207], v[36:39], v[84:87]\n\t"
    "s_waitcnt lgkmcnt(6)\n\t"
    "v_and_b32 v227, s53, v227\n\t"
    "v_mul_f32 v25, s52, v25\n\t"
    "v_mfma_f32_16x16x32_bf16 v[88:91], a[208:211], v[32:35], 0\n\t"
    "s_waitcnt lgkmcnt(4)\n\t"
    "v_mul_f32 v227, s52, v227\n\t"
    "v_cvt_pk_bf16_f32 v227, v25, v227\n\t"
    "v_mfma_f32_16x16x32_bf16 v[88:91], a[212:215], v[36:39], v[88:91]\n\t"
    "s_waitcnt lgkmcnt(2)\n\t"
    "v_accvgpr_write_b32 a35, v227\n\t"
    "v_lshlrev_b32 v24, 16, v228\n\t"
    "v_and_b32 v228, s53, v228\n\t"
    "v_mul_f32 v24, s52, v24\n\t"
    "v_mul_f32 v228, s52, v228\n\t"
    "v_cvt_pk_bf16_f32 v228, v24, v228\n\t"
    "v_accvgpr_write_b32 a36, v228\n\t"
    "v_lshlrev_b32 v25, 16, v229\n\t"
    "v_and_b32 v229, s53, v229\n\t"
    "v_mul_f32 v25, s52, v25\n\t"
    "v_mul_f32 v229, s52, v229\n\t"
    "v_cvt_pk_bf16_f32 v229, v25, v229\n\t"
    "v_accvgpr_write_b32 a37, v229\n\t"
    "v_lshlrev_b32 v24, 16, v230\n\t"
    "v_and_b32 v230, s53, v230\n\t"
    "v_mul_f32 v24, s52, v24\n\t"
    "v_mul_f32 v230, s52, v230\n\t"
    "v_cvt_pk_bf16_f32 v230, v24, v230\n\t"
    "v_accvgpr_write_b32 a38, v230\n\t"
    "v_lshlrev_b32 v25, 16, v231\n\t"
    "v_and_b32 v231, s53, v231\n\t"
    "v_mul_f32 v25, s52, v25\n\t"
    "v_mul_f32 v231, s52, v231\n\t"
    "v_cvt_pk_bf16_f32 v231, v25, v231\n\t"
    "v_accvgpr_write_b32 a39, v231\n\t"
    "v_lshlrev_b32 v24, 16, v232\n\t"
    "v_and_b32 v232, s53, v232\n\t"
    "v_mul_f32 v24, s52, v24\n\t"
    "v_mul_f32 v232, s52, v232\n\t"
    "v_cvt_pk_bf16_f32 v232, v24, v232\n\t"
    "v_accvgpr_write_b32 a40, v232\n\t"
    "v_lshlrev_b32 v25, 16, v233\n\t"
    "v_and_b32 v233, s53, v233\n\t"
    "v_mul_f32 v25, s52, v25\n\t"
    "v_mul_f32 v233, s52, v233\n\t"
    "v_cvt_pk_bf16_f32 v233, v25, v233\n\t"
    "v_accvgpr_write_b32 a41, v233\n\t"
    "v_lshlrev_b32 v24, 16, v234\n\t"
    "v_and_b32 v234, s53, v234\n\t"
    "v_mul_f32 v24, s52, v24\n\t"
    "v_mul_f32 v234, s52, v234\n\t"
    "v_cvt_pk_bf16_f32 v234, v24, v234\n\t"
    "v_accvgpr_write_b32 a42, v234\n\t"
    "v_lshlrev_b32 v25, 16, v235\n\t"
    "v_and_b32 v235, s53, v235\n\t"
    "v_mul_f32 v25, s52, v25\n\t"
    "v_mul_f32 v235, s52, v235\n\t"
    "v_cvt_pk_bf16_f32 v235, v25, v235\n\t"
    "v_accvgpr_write_b32 a43, v235\n\t"
    "v_lshlrev_b32 v24, 16, v236\n\t"
    "v_and_b32 v236, s53, v236\n\t"
    "v_mul_f32 v24, s52, v24\n\t"
    "v_mul_f32 v236, s52, v236\n\t"
    "v_cvt_pk_bf16_f32 v236, v24, v236\n\t"
    "v_accvgpr_write_b32 a44, v236\n\t"
    "v_lshlrev_b32 v25, 16, v237\n\t"
    "v_and_b32 v237, s53, v237\n\t"
    "v_mul_f32 v25, s52, v25\n\t"
    "v_mul_f32 v237, s52, v237\n\t"
    "v_cvt_pk_bf16_f32 v237, v25, v237\n\t"
    "v_accvgpr_write_b32 a45, v237\n\t"
    "v_lshlrev_b32 v24, 16, v238\n\t"
    "v_and_b32 v238, s53, v238\n\t"
    "v_mul_f32 v24, s52, v24\n\t"
    "v_mul_f32 v238, s52, v238\n\t"
    "v_cvt_pk_bf16_f32 v238, v24, v238\n\t"
    "v_accvgpr_write_b32 a46, v238\n\t"
    "v_lshlrev_b32 v25, 16, v239\n\t"
    "v_and_b32 v239, s53, v239\n\t"
    "v_mul_f32 v25, s52, v25\n\t"
    "v_mul_f32 v239, s52, v239\n\t"
    "v_cvt_pk_bf16_f32 v239, v25, v239\n\t"
    "v_accvgpr_write_b32 a47, v239\n\t"
    "v_mfma_f32_16x16x32_bf16 v[92:95], a[216:219], v[32:35], 0\n\t"
    "s_waitcnt lgkmcnt(0)\n\t"
    "v_exp_f32 v48, v48\n\t"
    "v_exp_f32 v49, v49\n\t"
    "v_exp_f32 v50, v50\n\t"
    "v_exp_f32 v51, v51\n\t"
    "v_mfma_f32_16x16x32_bf16 v[92:95], a[220:223], v[36:39], v[92:95]\n\t"
    "v_cvt_pk_bf16_f32 v40, v48, v49\n\t"
    "v_cvt_pk_bf16_f32 v41, v50, v51\n\t"
    "v_xor_b32 v1, 0x4000, v1\n\t"
    "v_xor_b32 v2, 0x4000, v2\n\t"
    "v_mfma_f32_16x16x32_bf16 v[48:51], a[96:99], a[32:35], 0\n\t"
    "s_waitcnt lgkmcnt(0)\n\t"
    "s_barrier\n\t"
    "s_waitcnt vmcnt(8)\n\t"
    "s_add_u32 m0, s59, 16384\n\t"
    "ds_read_b128 v[240:243], v6 offset:12288\n\t"
    "ds_read_b128 v[244:247], v6 offset:13312\n\t"
    "v_mfma_f32_16x16x32_bf16 v[48:51], a[100:103], a[36:39], v[48:51]\n\t"
    "buffer_load_dwordx4 v4, s[36:39], s68 offen offset:0 lds\n\t"
    "s_add_u32 m0, s59, 17344\n\t"
    "ds_read_b128 v[248:251], v6 offset:14336\n\t"
    "v_mfma_f32_16x16x32_bf16 v[48:51], a[104:107], a[40:43], v[48:51]\n\t"
    "buffer_load_dwordx4 v4, s[36:39], s68 offen offset:64 lds\n\t"
    "s_add_u32 m0, s59, 18304\n\t"
    "ds_read_b128 v[252:255], v6 offset:15360\n\t"
    "v_exp_f32 v52, v52\n\t"
    "v_exp_f32 v53, v53\n\t"
    "v_exp_f32 v54, v54\n\t"
    "v_exp_f32 v55, v55\n\t"
    "v_mfma_f32_16x16x32_bf16 v[48:51], a[108:111], a[44:47], v[48:51]\n\t"
    "buffer_load_dwordx4 v4, s[36:39], s68 offen offset:128 lds\n\t"
    "s_add_u32 m0, s59, 19264\n\t"
    "v_cvt_pk_bf16_f32 v42, v52, v53\n\t"
    "v_cvt_pk_bf16_f32 v43, v54, v55\n\t"
    "v_mfma_f32_16x16x32_bf16 v[52:55], a[112:115], a[32:35], 0\n\t"
    "buffer_load_dwordx4 v4, s[36:39], s68 offen offset:192 lds\n\t"
    "v_mfma_f32_16x16x32_bf16 v[52:55], a[116:119], a[36:39], v[52:55]\n\t"
    "v_exp_f32 v56, v56\n\t"
    "v_mfma_f32_16x16x32_bf16 v[52:55], a[120:123], a[40:43], v[52:55]\n\t"
    "s_waitcnt lgkmcnt(3)\n\t"
    "v_lshlrev_b32 v24, 16, v240\n\t"
    "v_and_b32 v240, s53, v240\n\t"
    "v_exp_f32 v57, v57\n\t"
    "v_exp_f32 v58, v58\n\t"
    "v_exp_f32 v59, v59\n\t"
    "v_mfma_f32_16x16x32_bf16 v[52:55], a[124:127], a[44:47], v[52:55]\n\t"
    "v_mul_f32 v24, s52, v24\n\t"
    "v_mul_f32 v240, s52, v240\n\t"
    "v_cvt_pk_bf16_f32 v44, v56, v57\n\t"
    "v_cvt_pk_bf16_f32 v45, v58, v59\n\t"
    "v_mfma_f32_16x16x32_bf16 v[56:59], a[128:131], a[32:35], 0\n\t"
    "v_cvt_pk_bf16_f32 v240, v24, v240\n\t"
    "v_accvgpr_write_b32 a48, v240\n\t"
    "v_mfma_f32_16x16x32_bf16 v[56:59], a[132:135], a[36:39], v[56:59]\n\t"
    "v_lshlrev_b32 v25, 16, v241\n\t"
    "v_and_b32 v241, s53, v241\n\t"
    "v_mfma_f32_16x16x32_bf16 v[56:59], a[136:139], a[40:43], v[56:59]\n\t"
    "v_mul_f32 v25, s52, v25\n\t"
    "v_mul_f32 v241, s52, v241\n\t"
    "v_exp_f32 v60, v60\n\t"
    "v_exp_f32 v61, v61\n\t"
    "v_exp_f32 v62, v62\n\t"
    "v_exp_f32 v63, v63\n\t"
    "v_mfma_f32_16x16x32_bf16 v[56:59], a[140:143], a[44:47], v[56:59]\n\t"
    "v_cvt_pk_bf16_f32 v241, v25, v241\n\t"
    "v_accvgpr_write_b32 a49, v241\n\t"
    "v_cvt_pk_bf16_f32 v46, v60, v61\n\t"
    "v_cvt_pk_bf16_f32 v47, v62, v63\n\t"
    "v_mfma_f32_16x16x32_bf16 v[60:63], a[144:147], a[32:35], 0\n\t"
    "v_lshlrev_b32 v24, 16, v242\n\t"
    "v_and_b32 v242, s53, v242\n\t"
    "v_mfma_f32_16x16x32_bf16 v[60:63], a[148:151], a[36:39], v[60:63]\n\t"
    "v_mul_f32 v24, s52, v24\n\t"
    "v_mul_f32 v242, s52, v242\n\t"
    "v_mfma_f32_16x16x32_bf16 v[60:63], a[152:155], a[40:43], v[60:63]\n\t"
    "v_cvt_pk_bf16_f32 v242, v24, v242\n\t"
    "v_accvgpr_write_b32 a50, v242\n\t"
    "v_mfma_f32_16x16x32_bf16 v[60:63], a[156:159], a[44:47], v[60:63]\n\t"
    "v_lshlrev_b32 v25, 16, v243\n\t"
    "v_and_b32 v243, s53, v243\n\t"
    "v_smfmac_f32_16x16x64_bf16 a[228:231], a[248:251], v[40:47], v3\n\t"
    "v_mul_f32 v25, s52, v25\n\t"
    "v_mul_f32 v243, s52, v243\n\t"
    "v_mfma_f32_16x16x32_bf16 v[96:99], a[160:163], v[40:43], 0\n\t"
    "v_cvt_pk_bf16_f32 v243, v25, v243\n\t"
    "v_accvgpr_write_b32 a51, v243\n\t"
    "v_mfma_f32_16x16x32_bf16 v[96:99], a[164:167], v[44:47], v[96:99]\n\t"
    "s_waitcnt lgkmcnt(2)\n\t"
    "v_lshlrev_b32 v24, 16, v244\n\t"
    "v_and_b32 v244, s53, v244\n\t"
    "v_mfma_f32_16x16x32_bf16 v[100:103], a[168:171], v[40:43], 0\n\t"
    "v_mul_f32 v24, s52, v24\n\t"
    "v_mul_f32 v244, s52, v244\n\t"
    "v_mfma_f32_16x16x32_bf16 v[100:103], a[172:175], v[44:47], v[100:103]\n\t"
    "v_cvt_pk_bf16_f32 v244, v24, v244\n\t"
    "v_accvgpr_write_b32 a52, v244\n\t"
    "v_mfma_f32_16x16x32_bf16 v[104:107], a[176:179], v[40:43], 0\n\t"
    "v_lshlrev_b32 v25, 16, v245\n\t"
    "v_and_b32 v245, s53, v245\n\t"
    "v_mfma_f32_16x16x32_bf16 v[104:107], a[180:183], v[44:47], v[104:107]\n\t"
    "v_mul_f32 v25, s52, v25\n\t"
    "v_mul_f32 v245, s52, v245\n\t"
    "v_mfma_f32_16x16x32_bf16 v[108:111], a[184:187], v[40:43], 0\n\t"
    "v_cvt_pk_bf16_f32 v245, v25, v245\n\t"
    "v_accvgpr_write_b32 a53, v245\n\t"
    "v_mfma_f32_16x16x32_bf16 v[108:111], a[188:191], v[44:47], v[108:111]\n\t"
    "v_lshlrev_b32 v24, 16, v246\n\t"
    "v_and_b32 v246, s53, v246\n\t"
    "v_mfma_f32_16x16x32_bf16 v[112:115], a[192:195], v[40:43], 0\n\t"
    "v_mul_f32 v24, s52, v24\n\t"
    "v_mul_f32 v246, s52, v246\n\t"
    "v_mfma_f32_16x16x32_bf16 v[112:115], a[196:199], v[44:47], v[112:115]\n\t"
    "v_cvt_pk_bf16_f32 v246, v24, v246\n\t"
    "v_accvgpr_write_b32 a54, v246\n\t"
    "v_mfma_f32_16x16x32_bf16 v[116:119], a[200:203], v[40:43], 0\n\t"
    "v_lshlrev_b32 v25, 16, v247\n\t"
    "v_and_b32 v247, s53, v247\n\t"
    "v_mfma_f32_16x16x32_bf16 v[116:119], a[204:207], v[44:47], v[116:119]\n\t"
    "v_mul_f32 v25, s52, v25\n\t"
    "v_mul_f32 v247, s52, v247\n\t"
    "v_mfma_f32_16x16x32_bf16 v[120:123], a[208:211], v[40:43], 0\n\t"
    "v_cvt_pk_bf16_f32 v247, v25, v247\n\t"
    "v_accvgpr_write_b32 a55, v247\n\t"
    "v_mfma_f32_16x16x32_bf16 v[120:123], a[212:215], v[44:47], v[120:123]\n\t"
    "s_waitcnt lgkmcnt(1)\n\t"
    "v_lshlrev_b32 v24, 16, v248\n\t"
    "v_and_b32 v248, s53, v248\n\t"
    "v_mul_f32 v24, s52, v24\n\t"
    "v_mul_f32 v248, s52, v248\n\t"
    "v_cvt_pk_bf16_f32 v248, v24, v248\n\t"
    "v_accvgpr_write_b32 a56, v248\n\t"
    "v_lshlrev_b32 v25, 16, v249\n\t"
    "v_and_b32 v249, s53, v249\n\t"
    "v_mul_f32 v25, s52, v25\n\t"
    "v_mul_f32 v249, s52, v249\n\t"
    "v_cvt_pk_bf16_f32 v249, v25, v249\n\t"
    "v_accvgpr_write_b32 a57, v249\n\t"
    "v_lshlrev_b32 v24, 16, v250\n\t"
    "v_and_b32 v250, s53, v250\n\t"
    "v_mul_f32 v24, s52, v24\n\t"
    "v_mul_f32 v250, s52, v250\n\t"
    "v_cvt_pk_bf16_f32 v250, v24, v250\n\t"
    "v_accvgpr_write_b32 a58, v250\n\t"
    "v_lshlrev_b32 v25, 16, v251\n\t"
    "v_and_b32 v251, s53, v251\n\t"
    "v_mul_f32 v25, s52, v25\n\t"
    "v_mul_f32 v251, s52, v251\n\t"
    "v_cvt_pk_bf16_f32 v251, v25, v251\n\t"
    "v_accvgpr_write_b32 a59, v251\n\t"
    "s_waitcnt lgkmcnt(0)\n\t"
    "v_lshlrev_b32 v24, 16, v252\n\t"
    "v_and_b32 v252, s53, v252\n\t"
    "v_mul_f32 v24, s52, v24\n\t"
    "v_mul_f32 v252, s52, v252\n\t"
    "v_cvt_pk_bf16_f32 v252, v24, v252\n\t"
    "v_accvgpr_write_b32 a60, v252\n\t"
    "v_lshlrev_b32 v25, 16, v253\n\t"
    "v_and_b32 v253, s53, v253\n\t"
    "v_mul_f32 v25, s52, v25\n\t"
    "v_mul_f32 v253, s52, v253\n\t"
    "v_cvt_pk_bf16_f32 v253, v25, v253\n\t"
    "v_accvgpr_write_b32 a61, v253\n\t"
    "v_lshlrev_b32 v24, 16, v254\n\t"
    "v_and_b32 v254, s53, v254\n\t"
    "v_mul_f32 v24, s52, v24\n\t"
    "v_mul_f32 v254, s52, v254\n\t"
    "v_cvt_pk_bf16_f32 v254, v24, v254\n\t"
    "v_accvgpr_write_b32 a62, v254\n\t"
    "v_lshlrev_b32 v25, 16, v255\n\t"
    "v_and_b32 v255, s53, v255\n\t"
    "v_mul_f32 v25, s52, v25\n\t"
    "v_mul_f32 v255, s52, v255\n\t"
    "v_cvt_pk_bf16_f32 v255, v25, v255\n\t"
    "v_accvgpr_write_b32 a63, v255\n\t"
    "v_mfma_f32_16x16x32_bf16 v[124:127], a[216:219], v[40:43], 0\n\t"
    "v_exp_f32 v48, v48\n\t"
    "v_exp_f32 v49, v49\n\t"
    "v_exp_f32 v50, v50\n\t"
    "v_exp_f32 v51, v51\n\t"
    "v_mfma_f32_16x16x32_bf16 v[124:127], a[220:223], v[44:47], v[124:127]\n\t"
    "v_cvt_pk_bf16_f32 v32, v48, v49\n\t"
    "v_cvt_pk_bf16_f32 v33, v50, v51\n\t"
    "v_mfma_f32_16x16x32_bf16 v[48:51], a[96:99], a[48:51], 0\n\t"
    "s_waitcnt vmcnt(8)\n\t"
    "s_add_u32 m0, s59, 49152\n\t"
    "ds_read_b128 v[224:227], v6 offset:16384\n\t"
    "ds_read_b128 v[228:231], v6 offset:17408\n\t"
    "v_mfma_f32_16x16x32_bf16 v[48:51], a[100:103], a[52:55], v[48:51]\n\t"
    "buffer_load_dwordx4 v5, s[40:43], s68 offen offset:0 lds\n\t"
    "buffer_load_dwordx4 v5, s[40:43], s68 offen offset:2048 lds\n\t"
    "s_add_u32 m0, s59, 50048\n\t"
    "v_mfma_f32_16x16x32_bf16 v[48:51], a[104:107], a[56:59], v[48:51]\n\t"
    "buffer_load_dwordx4 v5, s[40:43], s68 offen offset:128 lds\n\t"
    "buffer_load_dwordx4 v5, s[40:43], s68 offen offset:2176 lds\n\t"
    "v_exp_f32 v52, v52\n\t"
    "v_exp_f32 v53, v53\n\t"
    "v_exp_f32 v54, v54\n\t"
    "v_exp_f32 v55, v55\n\t"
    "v_mfma_f32_16x16x32_bf16 v[48:51], a[108:111], a[60:63], v[48:51]\n\t"
    "ds_read_b128 v[232:235], v6 offset:18432\n\t"
    "ds_read_b128 v[236:239], v6 offset:19456\n\t"
    "v_cvt_pk_bf16_f32 v34, v52, v53\n\t"
    "v_cvt_pk_bf16_f32 v35, v54, v55\n\t"
    "v_mfma_f32_16x16x32_bf16 v[52:55], a[112:115], a[48:51], 0\n\t"
    "v_exp_f32 v56, v56\n\t"
    "v_mfma_f32_16x16x32_bf16 v[52:55], a[116:119], a[52:55], v[52:55]\n\t"
    "v_exp_f32 v57, v57\n\t"
    "v_mfma_f32_16x16x32_bf16 v[52:55], a[120:123], a[56:59], v[52:55]\n\t"
    "s_waitcnt lgkmcnt(3)\n\t"
    "v_lshlrev_b32 v24, 16, v224\n\t"
    "v_and_b32 v224, s53, v224\n\t"
    "v_exp_f32 v58, v58\n\t"
    "v_exp_f32 v59, v59\n\t"
    "v_mfma_f32_16x16x32_bf16 v[52:55], a[124:127], a[60:63], v[52:55]\n\t"
    "v_mul_f32 v24, s52, v24\n\t"
    "v_mul_f32 v224, s52, v224\n\t"
    "v_cvt_pk_bf16_f32 v36, v56, v57\n\t"
    "v_cvt_pk_bf16_f32 v37, v58, v59\n\t"
    "v_mfma_f32_16x16x32_bf16 v[56:59], a[128:131], a[48:51], 0\n\t"
    "v_cvt_pk_bf16_f32 v224, v24, v224\n\t"
    "v_accvgpr_write_b32 a64, v224\n\t"
    "v_mfma_f32_16x16x32_bf16 v[56:59], a[132:135], a[52:55], v[56:59]\n\t"
    "v_lshlrev_b32 v25, 16, v225\n\t"
    "v_and_b32 v225, s53, v225\n\t"
    "v_mfma_f32_16x16x32_bf16 v[56:59], a[136:139], a[56:59], v[56:59]\n\t"
    "v_mul_f32 v25, s52, v25\n\t"
    "v_mul_f32 v225, s52, v225\n\t"
    "v_exp_f32 v60, v60\n\t"
    "v_exp_f32 v61, v61\n\t"
    "v_exp_f32 v62, v62\n\t"
    "v_exp_f32 v63, v63\n\t"
    "v_mfma_f32_16x16x32_bf16 v[56:59], a[140:143], a[60:63], v[56:59]\n\t"
    "v_cvt_pk_bf16_f32 v225, v25, v225\n\t"
    "v_accvgpr_write_b32 a65, v225\n\t"
    "v_cvt_pk_bf16_f32 v38, v60, v61\n\t"
    "v_cvt_pk_bf16_f32 v39, v62, v63\n\t"
    "v_mfma_f32_16x16x32_bf16 v[60:63], a[144:147], a[48:51], 0\n\t"
    "v_lshlrev_b32 v24, 16, v226\n\t"
    "v_and_b32 v226, s53, v226\n\t"
    "v_mfma_f32_16x16x32_bf16 v[60:63], a[148:151], a[52:55], v[60:63]\n\t"
    "v_mul_f32 v24, s52, v24\n\t"
    "v_mul_f32 v226, s52, v226\n\t"
    "v_mfma_f32_16x16x32_bf16 v[60:63], a[152:155], a[56:59], v[60:63]\n\t"
    "v_cvt_pk_bf16_f32 v226, v24, v226\n\t"
    "v_accvgpr_write_b32 a66, v226\n\t"
    "v_mfma_f32_16x16x32_bf16 v[60:63], a[156:159], a[60:63], v[60:63]\n\t"
    "v_lshlrev_b32 v25, 16, v227\n\t"
    "v_and_b32 v227, s53, v227\n\t"
    "v_smfmac_f32_16x16x64_bf16 a[232:235], a[248:251], v[32:39], v3\n\t"
    "v_mul_f32 v25, s52, v25\n\t"
    "v_mul_f32 v227, s52, v227\n\t"
    "v_mfma_f32_16x16x32_bf16 v[128:131], a[160:163], v[32:35], 0\n\t"
    "v_cvt_pk_bf16_f32 v227, v25, v227\n\t"
    "v_accvgpr_write_b32 a67, v227\n\t"
    "v_mfma_f32_16x16x32_bf16 v[128:131], a[164:167], v[36:39], v[128:131]\n\t"
    "s_waitcnt lgkmcnt(2)\n\t"
    "v_lshlrev_b32 v24, 16, v228\n\t"
    "v_and_b32 v228, s53, v228\n\t"
    "v_mfma_f32_16x16x32_bf16 v[132:135], a[168:171], v[32:35], 0\n\t"
    "v_mul_f32 v24, s52, v24\n\t"
    "v_mul_f32 v228, s52, v228\n\t"
    "v_mfma_f32_16x16x32_bf16 v[132:135], a[172:175], v[36:39], v[132:135]\n\t"
    "v_cvt_pk_bf16_f32 v228, v24, v228\n\t"
    "v_accvgpr_write_b32 a68, v228\n\t"
    "v_mfma_f32_16x16x32_bf16 v[136:139], a[176:179], v[32:35], 0\n\t"
    "v_lshlrev_b32 v25, 16, v229\n\t"
    "v_and_b32 v229, s53, v229\n\t"
    "v_mfma_f32_16x16x32_bf16 v[136:139], a[180:183], v[36:39], v[136:139]\n\t"
    "v_mul_f32 v25, s52, v25\n\t"
    "v_mul_f32 v229, s52, v229\n\t"
    "v_mfma_f32_16x16x32_bf16 v[140:143], a[184:187], v[32:35], 0\n\t"
    "v_cvt_pk_bf16_f32 v229, v25, v229\n\t"
    "v_accvgpr_write_b32 a69, v229\n\t"
    "v_mfma_f32_16x16x32_bf16 v[140:143], a[188:191], v[36:39], v[140:143]\n\t"
    "v_lshlrev_b32 v24, 16, v230\n\t"
    "v_and_b32 v230, s53, v230\n\t"
    "v_mfma_f32_16x16x32_bf16 v[144:147], a[192:195], v[32:35], 0\n\t"
    "v_mul_f32 v24, s52, v24\n\t"
    "v_mul_f32 v230, s52, v230\n\t"
    "v_mfma_f32_16x16x32_bf16 v[144:147], a[196:199], v[36:39], v[144:147]\n\t"
    "v_cvt_pk_bf16_f32 v230, v24, v230\n\t"
    "v_accvgpr_write_b32 a70, v230\n\t"
    "v_mfma_f32_16x16x32_bf16 v[148:151], a[200:203], v[32:35], 0\n\t"
    "v_lshlrev_b32 v25, 16, v231\n\t"
    "v_and_b32 v231, s53, v231\n\t"
    "v_mfma_f32_16x16x32_bf16 v[148:151], a[204:207], v[36:39], v[148:151]\n\t"
    "v_mul_f32 v25, s52, v25\n\t"
    "v_mul_f32 v231, s52, v231\n\t"
    "v_mfma_f32_16x16x32_bf16 v[152:155], a[208:211], v[32:35], 0\n\t"
    "v_cvt_pk_bf16_f32 v231, v25, v231\n\t"
    "v_accvgpr_write_b32 a71, v231\n\t"
    "v_mfma_f32_16x16x32_bf16 v[152:155], a[212:215], v[36:39], v[152:155]\n\t"
    "s_waitcnt lgkmcnt(1)\n\t"
    "v_lshlrev_b32 v24, 16, v232\n\t"
    "v_and_b32 v232, s53, v232\n\t"
    "v_mul_f32 v24, s52, v24\n\t"
    "v_mul_f32 v232, s52, v232\n\t"
    "v_cvt_pk_bf16_f32 v232, v24, v232\n\t"
    "v_accvgpr_write_b32 a72, v232\n\t"
    "v_lshlrev_b32 v25, 16, v233\n\t"
    "v_and_b32 v233, s53, v233\n\t"
    "v_mul_f32 v25, s52, v25\n\t"
    "v_mul_f32 v233, s52, v233\n\t"
    "v_cvt_pk_bf16_f32 v233, v25, v233\n\t"
    "v_accvgpr_write_b32 a73, v233\n\t"
    "v_lshlrev_b32 v24, 16, v234\n\t"
    "v_and_b32 v234, s53, v234\n\t"
    "v_mul_f32 v24, s52, v24\n\t"
    "v_mul_f32 v234, s52, v234\n\t"
    "v_cvt_pk_bf16_f32 v234, v24, v234\n\t"
    "v_accvgpr_write_b32 a74, v234\n\t"
    "v_lshlrev_b32 v25, 16, v235\n\t"
    "v_and_b32 v235, s53, v235\n\t"
    "v_mul_f32 v25, s52, v25\n\t"
    "v_mul_f32 v235, s52, v235\n\t"
    "v_cvt_pk_bf16_f32 v235, v25, v235\n\t"
    "v_accvgpr_write_b32 a75, v235\n\t"
    "s_waitcnt lgkmcnt(0)\n\t"
    "v_lshlrev_b32 v24, 16, v236\n\t"
    "v_and_b32 v236, s53, v236\n\t"
    "v_mul_f32 v24, s52, v24\n\t"
    "v_mul_f32 v236, s52, v236\n\t"
    "v_cvt_pk_bf16_f32 v236, v24, v236\n\t"
    "v_accvgpr_write_b32 a76, v236\n\t"
    "v_lshlrev_b32 v25, 16, v237\n\t"
    "v_and_b32 v237, s53, v237\n\t"
    "v_mul_f32 v25, s52, v25\n\t"
    "v_mul_f32 v237, s52, v237\n\t"
    "v_cvt_pk_bf16_f32 v237, v25, v237\n\t"
    "v_accvgpr_write_b32 a77, v237\n\t"
    "v_lshlrev_b32 v24, 16, v238\n\t"
    "v_and_b32 v238, s53, v238\n\t"
    "v_mul_f32 v24, s52, v24\n\t"
    "v_mul_f32 v238, s52, v238\n\t"
    "v_cvt_pk_bf16_f32 v238, v24, v238\n\t"
    "v_accvgpr_write_b32 a78, v238\n\t"
    "v_lshlrev_b32 v25, 16, v239\n\t"
    "v_and_b32 v239, s53, v239\n\t"
    "v_mul_f32 v25, s52, v25\n\t"
    "v_mul_f32 v239, s52, v239\n\t"
    "v_cvt_pk_bf16_f32 v239, v25, v239\n\t"
    "v_accvgpr_write_b32 a79, v239\n\t"
    "v_mfma_f32_16x16x32_bf16 v[156:159], a[216:219], v[32:35], 0\n\t"
    "v_exp_f32 v48, v48\n\t"
    "v_exp_f32 v49, v49\n\t"
    "v_exp_f32 v50, v50\n\t"
    "v_exp_f32 v51, v51\n\t"
    "v_mfma_f32_16x16x32_bf16 v[156:159], a[220:223], v[36:39], v[156:159]\n\t"
    "v_cvt_pk_bf16_f32 v40, v48, v49\n\t"
    "v_cvt_pk_bf16_f32 v41, v50, v51\n\t"
    "v_mfma_f32_16x16x32_bf16 v[48:51], a[96:99], a[64:67], 0\n\t"
    "s_waitcnt vmcnt(8)\n\t"
    "s_add_u32 s58, s58, 32768\n\t"
    "s_add_u32 m0, s59, 0\n\t"
    "ds_read_b128 v[240:243], v6 offset:20480\n\t"
    "ds_read_b128 v[244:247], v6 offset:21504\n\t"
    "v_mfma_f32_16x16x32_bf16 v[48:51], a[100:103], a[68:71], v[48:51]\n\t"
    "buffer_load_dwordx4 v4, s[36:39], s58 offen offset:0 lds\n\t"
    "s_add_u32 m0, s59, 960\n\t"
    "ds_read_b128 v[248:251], v6 offset:22528\n\t"
    "v_mfma_f32_16x16x32_bf16 v[48:51], a[104:107], a[72:75], v[48:51]\n\t"
    "buffer_load_dwordx4 v4, s[36:39], s58 offen offset:64 lds\n\t"
    "s_add_u32 m0, s59, 1920\n\t"
    "ds_read_b128 v[252:255], v6 offset:23552\n\t"
    "v_exp_f32 v52, v52\n\t"
    "v_exp_f32 v53, v53\n\t"
    "v_exp_f32 v54, v54\n\t"
    "v_exp_f32 v55, v55\n\t"
    "v_mfma_f32_16x16x32_bf16 v[48:51], a[108:111], a[76:79], v[48:51]\n\t"
    "buffer_load_dwordx4 v4, s[36:39], s58 offen offset:128 lds\n\t"
    "s_add_u32 m0, s59, 2880\n\t"
    "v_cvt_pk_bf16_f32 v42, v52, v53\n\t"
    "v_cvt_pk_bf16_f32 v43, v54, v55\n\t"
    "v_mfma_f32_16x16x32_bf16 v[52:55], a[112:115], a[64:67], 0\n\t"
    "buffer_load_dwordx4 v4, s[36:39], s58 offen offset:192 lds\n\t"
    "v_mfma_f32_16x16x32_bf16 v[52:55], a[116:119], a[68:71], v[52:55]\n\t"
    "v_exp_f32 v56, v56\n\t"
    "v_mfma_f32_16x16x32_bf16 v[52:55], a[120:123], a[72:75], v[52:55]\n\t"
    "s_waitcnt lgkmcnt(3)\n\t"
    "v_lshlrev_b32 v24, 16, v240\n\t"
    "v_and_b32 v240, s53, v240\n\t"
    "v_exp_f32 v57, v57\n\t"
    "v_exp_f32 v58, v58\n\t"
    "v_exp_f32 v59, v59\n\t"
    "v_mfma_f32_16x16x32_bf16 v[52:55], a[124:127], a[76:79], v[52:55]\n\t"
    "v_mul_f32 v24, s52, v24\n\t"
    "v_mul_f32 v240, s52, v240\n\t"
    "v_cvt_pk_bf16_f32 v44, v56, v57\n\t"
    "v_cvt_pk_bf16_f32 v45, v58, v59\n\t"
    "v_mfma_f32_16x16x32_bf16 v[56:59], a[128:131], a[64:67], 0\n\t"
    "v_cvt_pk_bf16_f32 v240, v24, v240\n\t"
    "v_accvgpr_write_b32 a80, v240\n\t"
    "v_mfma_f32_16x16x32_bf16 v[56:59], a[132:135], a[68:71], v[56:59]\n\t"
    "v_lshlrev_b32 v25, 16, v241\n\t"
    "v_and_b32 v241, s53, v241\n\t"
    "v_mfma_f32_16x16x32_bf16 v[56:59], a[136:139], a[72:75], v[56:59]\n\t"
    "v_mul_f32 v25, s52, v25\n\t"
    "v_mul_f32 v241, s52, v241\n\t"
    "v_exp_f32 v60, v60\n\t"
    "v_exp_f32 v61, v61\n\t"
    "v_exp_f32 v62, v62\n\t"
    "v_exp_f32 v63, v63\n\t"
    "v_mfma_f32_16x16x32_bf16 v[56:59], a[140:143], a[76:79], v[56:59]\n\t"
    "v_cvt_pk_bf16_f32 v241, v25, v241\n\t"
    "v_accvgpr_write_b32 a81, v241\n\t"
    "v_cvt_pk_bf16_f32 v46, v60, v61\n\t"
    "v_cvt_pk_bf16_f32 v47, v62, v63\n\t"
    "v_mfma_f32_16x16x32_bf16 v[60:63], a[144:147], a[64:67], 0\n\t"
    "v_lshlrev_b32 v24, 16, v242\n\t"
    "v_and_b32 v242, s53, v242\n\t"
    "v_mfma_f32_16x16x32_bf16 v[60:63], a[148:151], a[68:71], v[60:63]\n\t"
    "v_mul_f32 v24, s52, v24\n\t"
    "v_mul_f32 v242, s52, v242\n\t"
    "v_mfma_f32_16x16x32_bf16 v[60:63], a[152:155], a[72:75], v[60:63]\n\t"
    "v_cvt_pk_bf16_f32 v242, v24, v242\n\t"
    "v_accvgpr_write_b32 a82, v242\n\t"
    "v_mfma_f32_16x16x32_bf16 v[60:63], a[156:159], a[76:79], v[60:63]\n\t"
    "v_lshlrev_b32 v25, 16, v243\n\t"
    "v_and_b32 v243, s53, v243\n\t"
    "v_smfmac_f32_16x16x64_bf16 a[236:239], a[248:251], v[40:47], v3\n\t"
    "v_mul_f32 v25, s52, v25\n\t"
    "v_mul_f32 v243, s52, v243\n\t"
    "v_mfma_f32_16x16x32_bf16 v[160:163], a[160:163], v[40:43], 0\n\t"
    "v_cvt_pk_bf16_f32 v243, v25, v243\n\t"
    "v_accvgpr_write_b32 a83, v243\n\t"
    "v_mfma_f32_16x16x32_bf16 v[160:163], a[164:167], v[44:47], v[160:163]\n\t"
    "s_waitcnt lgkmcnt(2)\n\t"
    "v_lshlrev_b32 v24, 16, v244\n\t"
    "v_and_b32 v244, s53, v244\n\t"
    "v_mfma_f32_16x16x32_bf16 v[164:167], a[168:171], v[40:43], 0\n\t"
    "v_mul_f32 v24, s52, v24\n\t"
    "v_mul_f32 v244, s52, v244\n\t"
    "v_mfma_f32_16x16x32_bf16 v[164:167], a[172:175], v[44:47], v[164:167]\n\t"
    "v_cvt_pk_bf16_f32 v244, v24, v244\n\t"
    "v_accvgpr_write_b32 a84, v244\n\t"
    "v_mfma_f32_16x16x32_bf16 v[168:171], a[176:179], v[40:43], 0\n\t"
    "v_lshlrev_b32 v25, 16, v245\n\t"
    "v_and_b32 v245, s53, v245\n\t"
    "v_mfma_f32_16x16x32_bf16 v[168:171], a[180:183], v[44:47], v[168:171]\n\t"
    "v_mul_f32 v25, s52, v25\n\t"
    "v_mul_f32 v245, s52, v245\n\t"
    "v_mfma_f32_16x16x32_bf16 v[172:175], a[184:187], v[40:43], 0\n\t"
    "v_cvt_pk_bf16_f32 v245, v25, v245\n\t"
    "v_accvgpr_write_b32 a85, v245\n\t"
    "v_mfma_f32_16x16x32_bf16 v[172:175], a[188:191], v[44:47], v[172:175]\n\t"
    "v_lshlrev_b32 v24, 16, v246\n\t"
    "v_and_b32 v246, s53, v246\n\t"
    "v_mfma_f32_16x16x32_bf16 v[176:179], a[192:195], v[40:43], 0\n\t"
    "v_mul_f32 v24, s52, v24\n\t"
    "v_mul_f32 v246, s52, v246\n\t"
    "v_mfma_f32_16x16x32_bf16 v[176:179], a[196:199], v[44:47], v[176:179]\n\t"
    "v_cvt_pk_bf16_f32 v246, v24, v246\n\t"
    "v_accvgpr_write_b32 a86, v246\n\t"
    "v_mfma_f32_16x16x32_bf16 v[180:183], a[200:203], v[40:43], 0\n\t"
    "v_lshlrev_b32 v25, 16, v247\n\t"
    "v_and_b32 v247, s53, v247\n\t"
    "v_mfma_f32_16x16x32_bf16 v[180:183], a[204:207], v[44:47], v[180:183]\n\t"
    "v_mul_f32 v25, s52, v25\n\t"
    "v_mul_f32 v247, s52, v247\n\t"
    "v_mfma_f32_16x16x32_bf16 v[184:187], a[208:211], v[40:43], 0\n\t"
    "v_cvt_pk_bf16_f32 v247, v25, v247\n\t"
    "v_accvgpr_write_b32 a87, v247\n\t"
    "v_mfma_f32_16x16x32_bf16 v[184:187], a[212:215], v[44:47], v[184:187]\n\t"
    "s_waitcnt lgkmcnt(1)\n\t"
    "v_lshlrev_b32 v24, 16, v248\n\t"
    "v_and_b32 v248, s53, v248\n\t"
    "v_mul_f32 v24, s52, v24\n\t"
    "v_mul_f32 v248, s52, v248\n\t"
    "v_cvt_pk_bf16_f32 v248, v24, v248\n\t"
    "v_accvgpr_write_b32 a88, v248\n\t"
    "v_lshlrev_b32 v25, 16, v249\n\t"
    "v_and_b32 v249, s53, v249\n\t"
    "v_mul_f32 v25, s52, v25\n\t"
    "v_mul_f32 v249, s52, v249\n\t"
    "v_cvt_pk_bf16_f32 v249, v25, v249\n\t"
    "v_accvgpr_write_b32 a89, v249\n\t"
    "v_lshlrev_b32 v24, 16, v250\n\t"
    "v_and_b32 v250, s53, v250\n\t"
    "v_mul_f32 v24, s52, v24\n\t"
    "v_mul_f32 v250, s52, v250\n\t"
    "v_cvt_pk_bf16_f32 v250, v24, v250\n\t"
    "v_accvgpr_write_b32 a90, v250\n\t"
    "v_lshlrev_b32 v25, 16, v251\n\t"
    "v_and_b32 v251, s53, v251\n\t"
    "v_mul_f32 v25, s52, v25\n\t"
    "v_mul_f32 v251, s52, v251\n\t"
    "v_cvt_pk_bf16_f32 v251, v25, v251\n\t"
    "v_accvgpr_write_b32 a91, v251\n\t"
    "s_waitcnt lgkmcnt(0)\n\t"
    "v_lshlrev_b32 v24, 16, v252\n\t"
    "v_and_b32 v252, s53, v252\n\t"
    "v_mul_f32 v24, s52, v24\n\t"
    "v_mul_f32 v252, s52, v252\n\t"
    "v_cvt_pk_bf16_f32 v252, v24, v252\n\t"
    "v_accvgpr_write_b32 a92, v252\n\t"
    "v_lshlrev_b32 v25, 16, v253\n\t"
    "v_and_b32 v253, s53, v253\n\t"
    "v_mul_f32 v25, s52, v25\n\t"
    "v_mul_f32 v253, s52, v253\n\t"
    "v_cvt_pk_bf16_f32 v253, v25, v253\n\t"
    "v_accvgpr_write_b32 a93, v253\n\t"
    "v_lshlrev_b32 v24, 16, v254\n\t"
    "v_and_b32 v254, s53, v254\n\t"
    "v_mul_f32 v24, s52, v24\n\t"
    "v_mul_f32 v254, s52, v254\n\t"
    "v_cvt_pk_bf16_f32 v254, v24, v254\n\t"
    "v_accvgpr_write_b32 a94, v254\n\t"
    "v_lshlrev_b32 v25, 16, v255\n\t"
    "v_and_b32 v255, s53, v255\n\t"
    "v_mul_f32 v25, s52, v25\n\t"
    "v_mul_f32 v255, s52, v255\n\t"
    "v_cvt_pk_bf16_f32 v255, v25, v255\n\t"
    "v_accvgpr_write_b32 a95, v255\n\t"
    "v_mfma_f32_16x16x32_bf16 v[188:191], a[216:219], v[40:43], 0\n\t"
    "s_waitcnt vmcnt(4)\n\t"
    "s_add_u32 m0, s59, 32768\n\t"
    "v_exp_f32 v48, v48\n\t"
    "v_exp_f32 v49, v49\n\t"
    "v_exp_f32 v50, v50\n\t"
    "v_exp_f32 v51, v51\n\t"
    "v_mfma_f32_16x16x32_bf16 v[188:191], a[220:223], v[44:47], v[188:191]\n\t"
    "s_barrier\n\t"
    "buffer_load_dwordx4 v5, s[40:43], s58 offen offset:0 lds\n\t"
    "buffer_load_dwordx4 v5, s[40:43], s58 offen offset:2048 lds\n\t"
    "s_add_u32 m0, s59, 33664\n\t"
    "v_cvt_pk_bf16_f32 v32, v48, v49\n\t"
    "v_cvt_pk_bf16_f32 v33, v50, v51\n\t"
    "v_mfma_f32_16x16x32_bf16 v[48:51], a[96:99], a[80:83], 0\n\t"
    "buffer_load_dwordx4 v5, s[40:43], s58 offen offset:128 lds\n\t"
    "buffer_load_dwordx4 v5, s[40:43], s58 offen offset:2176 lds\n\t"
    "v_mfma_f32_16x16x32_bf16 v[48:51], a[100:103], a[84:87], v[48:51]\n\t"
    "v_exp_f32 v52, v52\n\t"
    "v_mfma_f32_16x16x32_bf16 v[48:51], a[104:107], a[88:91], v[48:51]\n\t"
    "v_exp_f32 v53, v53\n\t"
    "v_exp_f32 v54, v54\n\t"
    "v_exp_f32 v55, v55\n\t"
    "v_mfma_f32_16x16x32_bf16 v[48:51], a[108:111], a[92:95], v[48:51]\n\t"
    "ds_read_b128 a[96:99], v0 offset:0\n\t"
    "v_cvt_pk_bf16_f32 v34, v52, v53\n\t"
    "v_cvt_pk_bf16_f32 v35, v54, v55\n\t"
    "v_mfma_f32_16x16x32_bf16 v[52:55], a[112:115], a[80:83], 0\n\t"
    "v_exp_f32 v56, v56\n\t"
    "v_mfma_f32_16x16x32_bf16 v[52:55], a[116:119], a[84:87], v[52:55]\n\t"
    "v_exp_f32 v57, v57\n\t"
    "v_mfma_f32_16x16x32_bf16 v[52:55], a[120:123], a[88:91], v[52:55]\n\t"
    "v_exp_f32 v58, v58\n\t"
    "v_exp_f32 v59, v59\n\t"
    "v_mfma_f32_16x16x32_bf16 v[52:55], a[124:127], a[92:95], v[52:55]\n\t"
    "ds_read_b128 a[100:103], v0 offset:1024\n\t"
    "v_cvt_pk_bf16_f32 v36, v56, v57\n\t"
    "v_cvt_pk_bf16_f32 v37, v58, v59\n\t"
    "v_mfma_f32_16x16x32_bf16 v[56:59], a[128:131], a[80:83], 0\n\t"
    "v_exp_f32 v60, v60\n\t"
    "v_mfma_f32_16x16x32_bf16 v[56:59], a[132:135], a[84:87], v[56:59]\n\t"
    "v_exp_f32 v61, v61\n\t"
    "v_mfma_f32_16x16x32_bf16 v[56:59], a[136:139], a[88:91], v[56:59]\n\t"
    "v_exp_f32 v62, v62\n\t"
    "v_exp_f32 v63, v63\n\t"
    "v_mfma_f32_16x16x32_bf16 v[56:59], a[140:143], a[92:95], v[56:59]\n\t"
    "ds_read_b128 a[104:107], v0 offset:2048\n\t"
    "v_cvt_pk_bf16_f32 v38, v60, v61\n\t"
    "v_cvt_pk_bf16_f32 v39, v62, v63\n\t"
    "v_mfma_f32_16x16x32_bf16 v[60:63], a[144:147], a[80:83], 0\n\t"
    "v_exp_f32 v48, v48\n\t"
    "v_mfma_f32_16x16x32_bf16 v[60:63], a[148:151], a[84:87], v[60:63]\n\t"
    "v_exp_f32 v49, v49\n\t"
    "v_mfma_f32_16x16x32_bf16 v[60:63], a[152:155], a[88:91], v[60:63]\n\t"
    "v_exp_f32 v50, v50\n\t"
    "v_mfma_f32_16x16x32_bf16 v[60:63], a[156:159], a[92:95], v[60:63]\n\t"
    "v_exp_f32 v51, v51\n\t"
    "v_smfmac_f32_16x16x64_bf16 a[240:243], a[248:251], v[32:39], v3\n\t"
    "ds_read_b128 a[108:111], v0 offset:3072\n\t"
    "v_cvt_pk_bf16_f32 v40, v48, v49\n\t"
    "v_mfma_f32_16x16x32_bf16 v[192:195], a[160:163], v[32:35], 0\n\t"
    "ds_read_b128 a[112:115], v0 offset:4096\n\t"
    "v_cvt_pk_bf16_f32 v41, v50, v51\n\t"
    "v_mfma_f32_16x16x32_bf16 v[192:195], a[164:167], v[36:39], v[192:195]\n\t"
    "v_exp_f32 v52, v52\n\t"
    "v_mfma_f32_16x16x32_bf16 v[196:199], a[168:171], v[32:35], 0\n\t"
    "v_exp_f32 v53, v53\n\t"
    "v_mfma_f32_16x16x32_bf16 v[196:199], a[172:175], v[36:39], v[196:199]\n\t"
    "ds_read_b128 a[116:119], v0 offset:5120\n\t"
    "ds_read_b128 a[120:123], v0 offset:6144\n\t"
    "v_mfma_f32_16x16x32_bf16 v[200:203], a[176:179], v[32:35], 0\n\t"
    "v_exp_f32 v54, v54\n\t"
    "v_mfma_f32_16x16x32_bf16 v[200:203], a[180:183], v[36:39], v[200:203]\n\t"
    "v_exp_f32 v55, v55\n\t"
    "v_mfma_f32_16x16x32_bf16 v[204:207], a[184:187], v[32:35], 0\n\t"
    "ds_read_b128 a[124:127], v0 offset:7168\n\t"
    "v_cvt_pk_bf16_f32 v42, v52, v53\n\t"
    "v_mfma_f32_16x16x32_bf16 v[204:207], a[188:191], v[36:39], v[204:207]\n\t"
    "ds_read_b128 a[128:131], v0 offset:8192\n\t"
    "v_cvt_pk_bf16_f32 v43, v54, v55\n\t"
    "v_mfma_f32_16x16x32_bf16 v[208:211], a[192:195], v[32:35], 0\n\t"
    "v_exp_f32 v56, v56\n\t"
    "v_mfma_f32_16x16x32_bf16 v[208:211], a[196:199], v[36:39], v[208:211]\n\t"
    "ds_read_b128 a[132:135], v0 offset:9216\n\t"
    "ds_read_b128 a[136:139], v0 offset:10240\n\t"
    "v_mfma_f32_16x16x32_bf16 v[212:215], a[200:203], v[32:35], 0\n\t"
    "v_exp_f32 v57, v57\n\t"
    "v_mfma_f32_16x16x32_bf16 v[212:215], a[204:207], v[36:39], v[212:215]\n\t"
    "v_exp_f32 v58, v58\n\t"
    "v_mfma_f32_16x16x32_bf16 v[216:219], a[208:211], v[32:35], 0\n\t"
    "ds_read_b128 a[140:143], v0 offset:11264\n\t"
    "ds_read_b128 a[144:147], v0 offset:12288\n\t"
    "v_mfma_f32_16x16x32_bf16 v[216:219], a[212:215], v[36:39], v[216:219]\n\t"
    "v_exp_f32 v59, v59\n\t"
    "v_mfma_f32_16x16x32_bf16 v[220:223], a[216:219], v[32:35], 0\n\t"
    "ds_read_b128 a[148:151], v0 offset:13312\n\t"
    "v_cvt_pk_bf16_f32 v44, v56, v57\n\t"
    "v_mfma_f32_16x16x32_bf16 v[220:223], a[220:223], v[36:39], v[220:223]\n\t"
    "s_waitcnt lgkmcnt(13)\n\t"
    "ds_read_b128 a[152:155], v0 offset:14336\n\t"
    "v_cvt_pk_bf16_f32 v45, v58, v59\n\t"
    "v_mfma_f32_16x16x32_bf16 v[48:51], a[96:99], a[0:3], 0\n\t"
    "s_waitcnt lgkmcnt(13)\n\t"
    "v_exp_f32 v60, v60\n\t"
    "v_mfma_f32_16x16x32_bf16 v[48:51], a[100:103], a[4:7], v[48:51]\n\t"
    "s_waitcnt lgkmcnt(12)\n\t"
    "v_exp_f32 v61, v61\n\t"
    "v_mfma_f32_16x16x32_bf16 v[48:51], a[104:107], a[8:11], v[48:51]\n\t"
    "s_waitcnt lgkmcnt(11)\n\t"
    "ds_read_b128 a[156:159], v0 offset:15360\n\t"
    "v_xor_b32 v0, 0x4000, v0\n\t"
    "v_mfma_f32_16x16x32_bf16 v[48:51], a[108:111], a[12:15], v[48:51]\n\t"
    "s_waitcnt lgkmcnt(11)\n\t"
    "v_exp_f32 v62, v62\n\t"
    "v_mfma_f32_16x16x32_bf16 v[52:55], a[112:115], a[0:3], 0\n\t"
    "s_waitcnt lgkmcnt(10)\n\t"
    "v_exp_f32 v63, v63\n\t"
    "v_mfma_f32_16x16x32_bf16 v[52:55], a[116:119], a[4:7], v[52:55]\n\t"
    "s_waitcnt lgkmcnt(9)\n\t"
    "v_cvt_pk_bf16_f32 v46, v60, v61\n\t"
    "v_cvt_pk_bf16_f32 v47, v62, v63\n\t"
    "v_mfma_f32_16x16x32_bf16 v[52:55], a[120:123], a[8:11], v[52:55]\n\t"
    "s_waitcnt lgkmcnt(8)\n\t"
    "v_exp_f32 v48, v48\n\t"
    "v_mfma_f32_16x16x32_bf16 v[52:55], a[124:127], a[12:15], v[52:55]\n\t"
    "s_waitcnt lgkmcnt(7)\n\t"
    "v_exp_f32 v49, v49\n\t"
    "v_mfma_f32_16x16x32_bf16 v[56:59], a[128:131], a[0:3], 0\n\t"
    "s_waitcnt lgkmcnt(6)\n\t"
    "v_exp_f32 v50, v50\n\t"
    "v_mfma_f32_16x16x32_bf16 v[56:59], a[132:135], a[4:7], v[56:59]\n\t"
    "s_waitcnt lgkmcnt(5)\n\t"
    "v_exp_f32 v51, v51\n\t"
    "v_mfma_f32_16x16x32_bf16 v[56:59], a[136:139], a[8:11], v[56:59]\n\t"
    "s_waitcnt lgkmcnt(4)\n\t"
    "v_cvt_pk_bf16_f32 v32, v48, v49\n\t"
    "v_cvt_pk_bf16_f32 v33, v50, v51\n\t"
    "v_mfma_f32_16x16x32_bf16 v[56:59], a[140:143], a[12:15], v[56:59]\n\t"
    "s_waitcnt lgkmcnt(3)\n\t"
    "s_add_u32 s60, s60, 98304\n\t"
    "s_add_u32 m0, s61, 0\n\t"
    "v_exp_f32 v52, v52\n\t"
    "v_mfma_f32_16x16x32_bf16 v[60:63], a[144:147], a[0:3], 0\n\t"
    "s_waitcnt lgkmcnt(2)\n\t"
    "buffer_load_dwordx4 v4, s[44:47], s60 offen offset:0 lds\n\t"
    "v_mfma_f32_16x16x32_bf16 v[60:63], a[148:151], a[4:7], v[60:63]\n\t"
    "s_waitcnt lgkmcnt(1)\n\t"
    "v_exp_f32 v53, v53\n\t"
    "v_mfma_f32_16x16x32_bf16 v[60:63], a[152:155], a[8:11], v[60:63]\n\t"
    "s_waitcnt lgkmcnt(0)\n\t"
    "v_exp_f32 v54, v54\n\t"
    "v_mfma_f32_16x16x32_bf16 v[60:63], a[156:159], a[12:15], v[60:63]\n\t"
    "s_add_u32 m0, s61, 960\n\t"
    "v_exp_f32 v55, v55\n\t"
    "v_smfmac_f32_16x16x64_bf16 a[244:247], a[248:251], v[40:47], v3\n\t"
    "buffer_load_dwordx4 v4, s[44:47], s60 offen offset:64 lds\n\t"
    "v_cvt_pk_bf16_f32 v34, v52, v53\n\t"
    "v_mfma_f32_16x16x32_bf16 v[224:227], a[160:163], v[40:43], 0\n\t"
    "s_add_u32 m0, s61, 1920\n\t"
    "ds_read_b64_tr_b16 a[160:161], v1 offset:0\n\t"
    "v_cvt_pk_bf16_f32 v35, v54, v55\n\t"
    "v_mfma_f32_16x16x32_bf16 v[224:227], a[164:167], v[44:47], v[224:227]\n\t"
    "buffer_load_dwordx4 v4, s[44:47], s60 offen offset:128 lds\n\t"
    "ds_read_b64_tr_b16 a[162:163], v1 offset:4096\n\t"
    "v_mfma_f32_16x16x32_bf16 v[228:231], a[168:171], v[40:43], 0\n\t"
    "v_exp_f32 v56, v56\n\t"
    "v_mfma_f32_16x16x32_bf16 v[228:231], a[172:175], v[44:47], v[228:231]\n\t"
    "v_exp_f32 v57, v57\n\t"
    "v_mfma_f32_16x16x32_bf16 v[232:235], a[176:179], v[40:43], 0\n\t"
    "s_add_u32 m0, s61, 2880\n\t"
    "v_exp_f32 v58, v58\n\t"
    "v_mfma_f32_16x16x32_bf16 v[232:235], a[180:183], v[44:47], v[232:235]\n\t"
    "buffer_load_dwordx4 v4, s[44:47], s60 offen offset:192 lds\n\t"
    "ds_read_b64_tr_b16 a[164:165], v1 offset:8192\n\t"
    "v_mfma_f32_16x16x32_bf16 v[236:239], a[184:187], v[40:43], 0\n\t"
    "s_add_u32 s60, s60, 4096\n\t"
    "v_exp_f32 v59, v59\n\t"
    "v_mfma_f32_16x16x32_bf16 v[236:239], a[188:191], v[44:47], v[236:239]\n\t"
    "s_add_u32 s61, s61, 4096\n\t"
    "ds_read_b64_tr_b16 a[166:167], v1 offset:12288\n\t"
    "v_cvt_pk_bf16_f32 v36, v56, v57\n\t"
    "v_mfma_f32_16x16x32_bf16 v[240:243], a[192:195], v[40:43], 0\n\t"
    "s_add_u32 s58, s58, 16384\n\t"
    "ds_read_b64_tr_b16 a[168:169], v2 offset:0\n\t"
    "v_cvt_pk_bf16_f32 v37, v58, v59\n\t"
    "v_mfma_f32_16x16x32_bf16 v[240:243], a[196:199], v[44:47], v[240:243]\n\t"
    "s_xor_b32 s59, s59, 0x4000\n\t"
    "v_exp_f32 v60, v60\n\t"
    "v_mfma_f32_16x16x32_bf16 v[244:247], a[200:203], v[40:43], 0\n\t"
    "s_mov_b32 s57, 1\n\t"
    "v_exp_f32 v61, v61\n\t"
    "v_mfma_f32_16x16x32_bf16 v[244:247], a[204:207], v[44:47], v[244:247]\n\t"
    "v_exp_f32 v62, v62\n\t"
    "v_mfma_f32_16x16x32_bf16 v[248:251], a[208:211], v[40:43], 0\n\t"
    "v_exp_f32 v63, v63\n\t"
    "v_mfma_f32_16x16x32_bf16 v[248:251], a[212:215], v[44:47], v[248:251]\n\t"
    "ds_read_b64_tr_b16 a[170:171], v2 offset:4096\n\t"
    "v_cvt_pk_bf16_f32 v38, v60, v61\n\t"
    "v_mfma_f32_16x16x32_bf16 v[252:255], a[216:219], v[40:43], 0\n\t"
    "ds_read_b64_tr_b16 a[172:173], v2 offset:8192\n\t"
    "v_cvt_pk_bf16_f32 v39, v62, v63\n\t"
    "v_mfma_f32_16x16x32_bf16 v[252:255], a[220:223], v[44:47], v[252:255]\n\t"
    "ds_read_b64_tr_b16 a[174:175], v2 offset:12288\n\t"
    "ds_read_b64_tr_b16 a[176:177], v1 offset:512\n\t"
    "v_mfma_f32_16x16x32_bf16 v[48:51], a[96:99], a[16:19], 0\n\t"
    "ds_read_b64_tr_b16 a[178:179], v1 offset:4608\n\t"
    "ds_read_b64_tr_b16 a[180:181], v1 offset:8704\n\t"
    "v_mfma_f32_16x16x32_bf16 v[48:51], a[100:103], a[20:23], v[48:51]\n\t"
    "ds_read_b64_tr_b16 a[182:183], v1 offset:12800\n\t"
    "ds_read_b64_tr_b16 a[184:185], v2 offset:512\n\t"
    "v_mfma_f32_16x16x32_bf16 v[48:51], a[104:107], a[24:27], v[48:51]\n\t"
    "ds_read_b64_tr_b16 a[186:187], v2 offset:4608\n\t"
    "ds_read_b64_tr_b16 a[188:189], v2 offset:8704\n\t"
    "v_mfma_f32_16x16x32_bf16 v[48:51], a[108:111], a[28:31], v[48:51]\n\t"
    "ds_read_b64_tr_b16 a[190:191], v2 offset:12800\n\t"
    "ds_read_b64_tr_b16 a[192:193], v1 offset:1024\n\t"
    "v_mfma_f32_16x16x32_bf16 v[52:55], a[112:115], a[16:19], 0\n\t"
    "ds_read_b64_tr_b16 a[194:195], v1 offset:5120\n\t"
    "ds_read_b64_tr_b16 a[196:197], v1 offset:9216\n\t"
    "v_mfma_f32_16x16x32_bf16 v[52:55], a[116:119], a[20:23], v[52:55]\n\t"
    "ds_read_b64_tr_b16 a[198:199], v1 offset:13312\n\t"
    "ds_read_b64_tr_b16 a[200:201], v2 offset:1024\n\t"
    "v_mfma_f32_16x16x32_bf16 v[52:55], a[120:123], a[24:27], v[52:55]\n\t"
    "v_exp_f32 v48, v48\n\t"
    "v_mfma_f32_16x16x32_bf16 v[52:55], a[124:127], a[28:31], v[52:55]\n\t"
    "v_exp_f32 v49, v49\n\t"
    "v_mfma_f32_16x16x32_bf16 v[56:59], a[128:131], a[16:19], 0\n\t"
    "v_exp_f32 v50, v50\n\t"
    "v_mfma_f32_16x16x32_bf16 v[56:59], a[132:135], a[20:23], v[56:59]\n\t"
    "ds_read_b64_tr_b16 a[202:203], v2 offset:5120\n\t"
    "ds_read_b64_tr_b16 a[204:205], v2 offset:9216\n\t"
    "v_mfma_f32_16x16x32_bf16 v[56:59], a[136:139], a[24:27], v[56:59]\n\t"
    "v_exp_f32 v51, v51\n\t"
    "v_mfma_f32_16x16x32_bf16 v[56:59], a[140:143], a[28:31], v[56:59]\n\t"
    "ds_read_b64_tr_b16 a[206:207], v2 offset:13312\n\t"
    "ds_read_b64_tr_b16 a[208:209], v1 offset:1536\n\t"
    "v_mfma_f32_16x16x32_bf16 v[60:63], a[144:147], a[16:19], 0\n\t"
    "ds_read_b64_tr_b16 a[210:211], v1 offset:5632\n\t"
    "v_cvt_pk_bf16_f32 v40, v48, v49\n\t"
    "v_mfma_f32_16x16x32_bf16 v[60:63], a[148:151], a[20:23], v[60:63]\n\t"
    "ds_read_b64_tr_b16 a[212:213], v1 offset:9728\n\t"
    "v_cvt_pk_bf16_f32 v41, v50, v51\n\t"
    "v_mfma_f32_16x16x32_bf16 v[60:63], a[152:155], a[24:27], v[60:63]\n\t"
    "v_exp_f32 v52, v52\n\t"
    "v_mfma_f32_16x16x32_bf16 v[60:63], a[156:159], a[28:31], v[60:63]\n\t"
    "ds_read_b64_tr_b16 a[214:215], v1 offset:13824\n\t"
    "ds_read_b64_tr_b16 a[216:217], v2 offset:1536\n\t"
    "v_smfmac_f32_16x16x64_bf16 a[224:227], a[248:251], v[32:39], v3\n\t"
    "s_waitcnt lgkmcnt(15)\n\t"
    "v_exp_f32 v53, v53\n\t"
    "v_mfma_f32_16x16x32_bf16 v[64:67], a[160:163], v[32:35], v[64:67]\n\t"
    "ds_read_b64_tr_b16 a[218:219], v2 offset:5632\n\t"
    "ds_read_b64_tr_b16 a[220:221], v2 offset:9728\n\t"
    "v_mfma_f32_16x16x32_bf16 v[64:67], a[164:167], v[36:39], v[64:67]\n\t"
    "v_exp_f32 v54, v54\n\t"
    "v_mfma_f32_16x16x32_bf16 v[68:71], a[168:171], v[32:35], v[68:71]\n\t"
    "ds_read_b64_tr_b16 a[222:223], v2 offset:13824\n\t"
    "v_xor_b32 v1, 0x4000, v1\n\t"
    "v_mfma_f32_16x16x32_bf16 v[68:71], a[172:175], v[36:39], v[68:71]\n\t"
    "v_exp_f32 v55, v55\n\t"
    "v_mfma_f32_16x16x32_bf16 v[72:75], a[176:179], v[32:35], v[72:75]\n\t"
    "v_cvt_pk_bf16_f32 v42, v52, v53\n\t"
    "v_cvt_pk_bf16_f32 v43, v54, v55\n\t"
    "v_mfma_f32_16x16x32_bf16 v[72:75], a[180:183], v[36:39], v[72:75]\n\t"
    "v_exp_f32 v56, v56\n\t"
    "v_mfma_f32_16x16x32_bf16 v[76:79], a[184:187], v[32:35], v[76:79]\n\t"
    "s_waitcnt lgkmcnt(15)\n\t"
    "v_exp_f32 v57, v57\n\t"
    "v_mfma_f32_16x16x32_bf16 v[76:79], a[188:191], v[36:39], v[76:79]\n\t"
    "s_waitcnt lgkmcnt(14)\n\t"
    "v_exp_f32 v58, v58\n\t"
    "v_mfma_f32_16x16x32_bf16 v[80:83], a[192:195], v[32:35], v[80:83]\n\t"
    "s_waitcnt lgkmcnt(12)\n\t"
    "v_exp_f32 v59, v59\n\t"
    "v_mfma_f32_16x16x32_bf16 v[80:83], a[196:199], v[36:39], v[80:83]\n\t"
    "s_waitcnt lgkmcnt(10)\n\t"
    "v_cvt_pk_bf16_f32 v44, v56, v57\n\t"
    "v_cvt_pk_bf16_f32 v45, v58, v59\n\t"
    "v_mfma_f32_16x16x32_bf16 v[84:87], a[200:203], v[32:35], v[84:87]\n\t"
    "s_waitcnt lgkmcnt(8)\n\t"
    "v_xor_b32 v2, 0x4000, v2\n\t"
    "v_mfma_f32_16x16x32_bf16 v[84:87], a[204:207], v[36:39], v[84:87]\n\t"
    "s_waitcnt lgkmcnt(6)\n\t"
    "v_mfma_f32_16x16x32_bf16 v[88:91], a[208:211], v[32:35], v[88:91]\n\t"
    "s_waitcnt lgkmcnt(4)\n\t"
    "v_mfma_f32_16x16x32_bf16 v[88:91], a[212:215], v[36:39], v[88:91]\n\t"
    "s_waitcnt lgkmcnt(2)\n\t"
    "v_mfma_f32_16x16x32_bf16 v[92:95], a[216:219], v[32:35], v[92:95]\n\t"
    "s_waitcnt lgkmcnt(0)\n\t"
    "v_mfma_f32_16x16x32_bf16 v[92:95], a[220:223], v[36:39], v[92:95]\n\t"
    ".p2align 4\n\t"
    "L_L0:\n\t"
    "v_mfma_f32_16x16x32_bf16 v[48:51], a[96:99], a[32:35], 0\n\t"
    "s_waitcnt vmcnt(0) lgkmcnt(0)\n\t"
    "s_barrier\n\t"
    "v_exp_f32 v60, v60\n\t"
    "v_mfma_f32_16x16x32_bf16 v[48:51], a[100:103], a[36:39], v[48:51]\n\t"
    "s_sub_u32 s69, s58, 32768\n\t"
    "s_cmp_eq_u32 s57, 6\n\t"
    "s_cselect_b32 s68, s69, s58\n\t"
    "s_cselect_b32 s84, s69, s60\n\t"
    "s_cselect_b32 s85, s59, s61\n\t"
    "s_cselect_b64 s[80:81], s[36:37], s[44:45]\n\t"
    "s_cselect_b64 s[82:83], s[38:39], s[46:47]\n\t"
    "v_exp_f32 v61, v61\n\t"
    "v_mfma_f32_16x16x32_bf16 v[48:51], a[104:107], a[40:43], v[48:51]\n\t"
    "s_add_u32 m0, s59, 0\n\t"
    "ds_read_b128 v[16:19], v6 offset:0\n\t"
    "ds_read_b128 v[20:23], v6 offset:1024\n\t"
    "v_mfma_f32_16x16x32_bf16 v[48:51], a[108:111], a[44:47], v[48:51]\n\t"
    "v_exp_f32 v62, v62\n\t"
    "v_mfma_f32_16x16x32_bf16 v[52:55], a[112:115], a[32:35], 0\n\t"
    "v_exp_f32 v63, v63\n\t"
    "v_mfma_f32_16x16x32_bf16 v[52:55], a[116:119], a[36:39], v[52:55]\n\t"
    "buffer_load_dwordx4 v4, s[36:39], s68 offen offset:0 lds\n\t"
    "v_cvt_pk_bf16_f32 v46, v60, v61\n\t"
    "v_mfma_f32_16x16x32_bf16 v[52:55], a[120:123], a[40:43], v[52:55]\n\t"
    "v_cvt_pk_bf16_f32 v47, v62, v63\n\t"
    "v_mfma_f32_16x16x32_bf16 v[52:55], a[124:127], a[44:47], v[52:55]\n\t"
    "v_exp_f32 v48, v48\n\t"
    "v_mfma_f32_16x16x32_bf16 v[56:59], a[128:131], a[32:35], 0\n\t"
    "s_waitcnt lgkmcnt(1)\n\t"
    "v_lshlrev_b32 v24, 16, v16\n\t"
    "v_and_b32 v16, s53, v16\n\t"
    "v_mfma_f32_16x16x32_bf16 v[56:59], a[132:135], a[36:39], v[56:59]\n\t"
    "s_add_u32 m0, s59, 960\n\t"
    "v_mul_f32 v24, s52, v24\n\t"
    "v_mul_f32 v16, s52, v16\n\t"
    "v_mfma_f32_16x16x32_bf16 v[56:59], a[136:139], a[40:43], v[56:59]\n\t"
    "buffer_load_dwordx4 v4, s[36:39], s68 offen offset:64 lds\n\t"
    "v_cvt_pk_bf16_f32 v16, v24, v16\n\t"
    "v_mfma_f32_16x16x32_bf16 v[56:59], a[140:143], a[44:47], v[56:59]\n\t"
    "v_lshlrev_b32 v25, 16, v17\n\t"
    "v_and_b32 v17, s53, v17\n\t"
    "v_mfma_f32_16x16x32_bf16 v[60:63], a[144:147], a[32:35], 0\n\t"
    "v_mul_f32 v25, s52, v25\n\t"
    "v_mul_f32 v17, s52, v17\n\t"
    "v_mfma_f32_16x16x32_bf16 v[60:63], a[148:151], a[36:39], v[60:63]\n\t"
    "v_cvt_pk_bf16_f32 v17, v25, v17\n\t"
    "v_lshlrev_b32 v24, 16, v18\n\t"
    "v_mfma_f32_16x16x32_bf16 v[60:63], a[152:155], a[40:43], v[60:63]\n\t"
    "v_exp_f32 v49, v49\n\t"
    "v_mfma_f32_16x16x32_bf16 v[60:63], a[156:159], a[44:47], v[60:63]\n\t"
    "v_exp_f32 v50, v50\n\t"
    "v_smfmac_f32_16x16x64_bf16 a[228:231], a[248:251], v[40:47], v3\n\t"
    "s_add_u32 m0, s59, 1920\n\t"
    "v_and_b32 v18, s53, v18\n\t"
    "v_mul_f32 v24, s52, v24\n\t"
    "v_mfma_f32_16x16x32_bf16 v[96:99], a[160:163], v[40:43], v[96:99]\n\t"
    "v_exp_f32 v51, v51\n\t"
    "v_mfma_f32_16x16x32_bf16 v[96:99], a[164:167], v[44:47], v[96:99]\n\t"
    "buffer_load_dwordx4 v4, s[36:39], s68 offen offset:128 lds\n\t"
    "v_cvt_pk_bf16_f32 v32, v48, v49\n\t"
    "v_mfma_f32_16x16x32_bf16 v[100:103], a[168:171], v[40:43], v[100:103]\n\t"
    "v_cvt_pk_bf16_f32 v33, v50, v51\n\t"
    "v_mul_f32 v18, s52, v18\n\t"
    "v_mfma_f32_16x16x32_bf16 v[100:103], a[172:175], v[44:47], v[100:103]\n\t"
    "v_exp_f32 v52, v52\n\t"
    "v_mfma_f32_16x16x32_bf16 v[104:107], a[176:179], v[40:43], v[104:107]\n\t"
    "v_exp_f32 v53, v53\n\t"
    "v_mfma_f32_16x16x32_bf16 v[104:107], a[180:183], v[44:47], v[104:107]\n\t"
    "v_exp_f32 v54, v54\n\t"
    "v_mfma_f32_16x16x32_bf16 v[108:111], a[184:187], v[40:43], v[108:111]\n\t"
    "s_add_u32 m0, s59, 2880\n\t"
    "v_exp_f32 v55, v55\n\t"
    "v_mfma_f32_16x16x32_bf16 v[108:111], a[188:191], v[44:47], v[108:111]\n\t"
    "buffer_load_dwordx4 v4, s[36:39], s68 offen offset:192 lds\n\t"
    "v_cvt_pk_bf16_f32 v34, v52, v53\n\t"
    "v_mfma_f32_16x16x32_bf16 v[112:115], a[192:195], v[40:43], v[112:115]\n\t"
    "v_cvt_pk_bf16_f32 v18, v24, v18\n\t"
    "v_cvt_pk_bf16_f32 v35, v54, v55\n\t"
    "v_mfma_f32_16x16x32_bf16 v[112:115], a[196:199], v[44:47], v[112:115]\n\t"
    "v_exp_f32 v56, v56\n\t"
    "v_mfma_f32_16x16x32_bf16 v[116:119], a[200:203], v[40:43], v[116:119]\n\t"
    "v_exp_f32 v57, v57\n\t"
    "v_mfma_f32_16x16x32_bf16 v[116:119], a[204:207], v[44:47], v[116:119]\n\t"
    "v_lshlrev_b32 v25, 16, v19\n\t"
    "v_and_b32 v19, s53, v19\n\t"
    "v_mfma_f32_16x16x32_bf16 v[120:123], a[208:211], v[40:43], v[120:123]\n\t"
    "v_exp_f32 v58, v58\n\t"
    "v_mfma_f32_16x16x32_bf16 v[120:123], a[212:215], v[44:47], v[120:123]\n\t"
    "s_add_u32 m0, s59, 32768\n\t"
    "v_exp_f32 v59, v59\n\t"
    "v_mfma_f32_16x16x32_bf16 v[124:127], a[216:219], v[40:43], v[124:127]\n\t"
    "buffer_load_dwordx4 v5, s[40:43], s68 offen offset:0 lds\n\t"
    "v_cvt_pk_bf16_f32 v36, v56, v57\n\t"
    "v_mfma_f32_16x16x32_bf16 v[124:127], a[220:223], v[44:47], v[124:127]\n\t"
    "v_cvt_pk_bf16_f32 v37, v58, v59\n\t"
    "v_mul_f32 v25, s52, v25\n\t"
    "v_mfma_f32_16x16x32_bf16 v[48:51], a[96:99], a[48:51], 0\n\t"
    "v_exp_f32 v60, v60\n\t"
    "v_mfma_f32_16x16x32_bf16 v[48:51], a[100:103], a[52:55], v[48:51]\n\t"
    "v_exp_f32 v61, v61\n\t"
    "v_mfma_f32_16x16x32_bf16 v[48:51], a[104:107], a[56:59], v[48:51]\n\t"
    "v_exp_f32 v62, v62\n\t"
    "v_mfma_f32_16x16x32_bf16 v[48:51], a[108:111], a[60:63], v[48:51]\n\t"
    "v_exp_f32 v63, v63\n\t"
    "v_mfma_f32_16x16x32_bf16 v[52:55], a[112:115], a[48:51], 0\n\t"
    "v_mul_f32 v19, s52, v19\n\t"
    "v_cvt_pk_bf16_f32 v38, v60, v61\n\t"
    "v_mfma_f32_16x16x32_bf16 v[52:55], a[116:119], a[52:55], v[52:55]\n\t"
    "buffer_load_dwordx4 v5, s[40:43], s68 offen offset:2048 lds\n\t"
    "v_cvt_pk_bf16_f32 v39, v62, v63\n\t"
    "v_mfma_f32_16x16x32_bf16 v[52:55], a[120:123], a[56:59], v[52:55]\n\t"
    "v_exp_f32 v48, v48\n\t"
    "v_mfma_f32_16x16x32_bf16 v[52:55], a[124:127], a[60:63], v[52:55]\n\t"
    "v_exp_f32 v49, v49\n\t"
    "v_mfma_f32_16x16x32_bf16 v[56:59], a[128:131], a[48:51], 0\n\t"
    "v_exp_f32 v50, v50\n\t"
    "v_mfma_f32_16x16x32_bf16 v[56:59], a[132:135], a[52:55], v[56:59]\n\t"
    "v_exp_f32 v51, v51\n\t"
    "v_mfma_f32_16x16x32_bf16 v[56:59], a[136:139], a[56:59], v[56:59]\n\t"
    "v_cvt_pk_bf16_f32 v19, v25, v19\n\t"
    "v_cvt_pk_bf16_f32 v40, v48, v49\n\t"
    "v_mfma_f32_16x16x32_bf16 v[56:59], a[140:143], a[60:63], v[56:59]\n\t"
    "s_add_u32 m0, s59, 33664\n\t"
    "ds_write_b128 v6, v[16:19] offset:0\n\t"
    "v_mfma_f32_16x16x32_bf16 v[60:63], a[144:147], a[48:51], 0\n\t"
    "buffer_load_dwordx4 v5, s[40:43], s68 offen offset:128 lds\n\t"
    "ds_read_b128 v[16:19], v6 offset:2048\n\t"
    "v_mfma_f32_16x16x32_bf16 v[60:63], a[148:151], a[52:55], v[60:63]\n\t"
    "s_waitcnt lgkmcnt(2)\n\t"
    "v_lshlrev_b32 v24, 16, v20\n\t"
    "v_and_b32 v20, s53, v20\n\t"
    "v_mfma_f32_16x16x32_bf16 v[60:63], a[152:155], a[56:59], v[60:63]\n\t"
    "v_mul_f32 v24, s52, v24\n\t"
    "v_mul_f32 v20, s52, v20\n\t"
    "v_mfma_f32_16x16x32_bf16 v[60:63], a[156:159], a[60:63], v[60:63]\n\t"
    "v_cvt_pk_bf16_f32 v41, v50, v51\n\t"
    "v_cvt_pk_bf16_f32 v20, v24, v20\n\t"
    "v_smfmac_f32_16x16x64_bf16 a[232:235], a[248:251], v[32:39], v3\n\t"
    "v_exp_f32 v52, v52\n\t"
    "v_mfma_f32_16x16x32_bf16 v[128:131], a[160:163], v[32:35], v[128:131]\n\t"
    "v_exp_f32 v53, v53\n\t"
    "v_mfma_f32_16x16x32_bf16 v[128:131], a[164:167], v[36:39], v[128:131]\n\t"
    "v_exp_f32 v54, v54\n\t"
    "v_mfma_f32_16x16x32_bf16 v[132:135], a[168:171], v[32:35], v[132:135]\n\t"
    "buffer_load_dwordx4 v5, s[40:43], s68 offen offset:2176 lds\n\t"
    "v_lshlrev_b32 v25, 16, v21\n\t"
    "v_mfma_f32_16x16x32_bf16 v[132:135], a[172:175], v[36:39], v[132:135]\n\t"
    "v_exp_f32 v55, v55\n\t"
    "v_mfma_f32_16x16x32_bf16 v[136:139], a[176:179], v[32:35], v[136:139]\n\t"
    "v_cvt_pk_bf16_f32 v42, v52, v53\n\t"
    "v_and_b32 v21, s53, v21\n\t"
    "v_mfma_f32_16x16x32_bf16 v[136:139], a[180:183], v[36:39], v[136:139]\n\t"
    "v_cvt_pk_bf16_f32 v43, v54, v55\n\t"
    "v_mul_f32 v25, s52, v25\n\t"
    "v_mfma_f32_16x16x32_bf16 v[140:143], a[184:187], v[32:35], v[140:143]\n\t"
    "v_exp_f32 v56, v56\n\t"
    "v_mfma_f32_16x16x32_bf16 v[140:143], a[188:191], v[36:39], v[140:143]\n\t"
    "v_exp_f32 v57, v57\n\t"
    "v_mfma_f32_16x16x32_bf16 v[144:147], a[192:195], v[32:35], v[144:147]\n\t"
    "s_add_u32 m0, s85, 0\n\t"
    "v_exp_f32 v58, v58\n\t"
    "v_mfma_f32_16x16x32_bf16 v[144:147], a[196:199], v[36:39], v[144:147]\n\t"
    "v_exp_f32 v59, v59\n\t"
    "v_mfma_f32_16x16x32_bf16 v[148:151], a[200:203], v[32:35], v[148:151]\n\t"
    "buffer_load_dwordx4 v4, s[80:83], s84 offen offset:0 lds\n\t"
    "v_mul_f32 v21, s52, v21\n\t"
    "v_mfma_f32_16x16x32_bf16 v[148:151], a[204:207], v[36:39], v[148:151]\n\t"
    "v_cvt_pk_bf16_f32 v44, v56, v57\n\t"
    "v_cvt_pk_bf16_f32 v45, v58, v59\n\t"
    "v_mfma_f32_16x16x32_bf16 v[152:155], a[208:211], v[32:35], v[152:155]\n\t"
    "v_exp_f32 v60, v60\n\t"
    "v_mfma_f32_16x16x32_bf16 v[152:155], a[212:215], v[36:39], v[152:155]\n\t"
    "v_cvt_pk_bf16_f32 v21, v25, v21\n\t"
    "v_lshlrev_b32 v24, 16, v22\n\t"
    "v_mfma_f32_16x16x32_bf16 v[156:159], a[216:219], v[32:35], v[156:159]\n\t"
    "v_exp_f32 v61, v61\n\t"
    "v_mfma_f32_16x16x32_bf16 v[156:159], a[220:223], v[36:39], v[156:159]\n\t"
    "s_add_u32 m0, s85, 960\n\t"
    "v_exp_f32 v62, v62\n\t"
    "v_mfma_f32_16x16x32_bf16 v[48:51], a[96:99], a[64:67], 0\n\t"
    "v_exp_f32 v63, v63\n\t"
    "v_mfma_f32_16x16x32_bf16 v[48:51], a[100:103], a[68:71], v[48:51]\n\t"
    "buffer_load_dwordx4 v4, s[80:83], s84 offen offset:64 lds\n\t"
    "v_cvt_pk_bf16_f32 v46, v60, v61\n\t"
    "v_mfma_f32_16x16x32_bf16 v[48:51], a[104:107], a[72:75], v[48:51]\n\t"
    "v_cvt_pk_bf16_f32 v47, v62, v63\n\t"
    "v_and_b32 v22, s53, v22\n\t"
    "v_mfma_f32_16x16x32_bf16 v[48:51], a[108:111], a[76:79], v[48:51]\n\t"
    "v_mul_f32 v24, s52, v24\n\t"
    "v_mul_f32 v22, s52, v22\n\t"
    "v_mfma_f32_16x16x32_bf16 v[52:55], a[112:115], a[64:67], 0\n\t"
    "v_cvt_pk_bf16_f32 v22, v24, v22\n\t"
    "v_lshlrev_b32 v25, 16, v23\n\t"
    "v_mfma_f32_16x16x32_bf16 v[52:55], a[116:119], a[68:71], v[52:55]\n\t"
    "v_and_b32 v23, s53, v23\n\t"
    "v_mul_f32 v25, s52, v25\n\t"
    "v_mfma_f32_16x16x32_bf16 v[52:55], a[120:123], a[72:75], v[52:55]\n\t"
    "s_add_u32 m0, s85, 1920\n\t"
    "v_exp_f32 v48, v48\n\t"
    "v_mfma_f32_16x16x32_bf16 v[52:55], a[124:127], a[76:79], v[52:55]\n\t"
    "v_exp_f32 v49, v49\n\t"
    "v_mfma_f32_16x16x32_bf16 v[56:59], a[128:131], a[64:67], 0\n\t"
    "v_exp_f32 v50, v50\n\t"
    "v_mfma_f32_16x16x32_bf16 v[56:59], a[132:135], a[68:71], v[56:59]\n\t"
    "buffer_load_dwordx4 v4, s[80:83], s84 offen offset:128 lds\n\t"
    "v_mul_f32 v23, s52, v23\n\t"
    "v_mfma_f32_16x16x32_bf16 v[56:59], a[136:139], a[72:75], v[56:59]\n\t"
    "v_exp_f32 v51, v51\n\t"
    "v_mfma_f32_16x16x32_bf16 v[56:59], a[140:143], a[76:79], v[56:59]\n\t"
    "v_cvt_pk_bf16_f32 v32, v48, v49\n\t"
    "v_cvt_pk_bf16_f32 v33, v50, v51\n\t"
    "v_mfma_f32_16x16x32_bf16 v[60:63], a[144:147], a[64:67], 0\n\t"
    "v_exp_f32 v52, v52\n\t"
    "v_mfma_f32_16x16x32_bf16 v[60:63], a[148:151], a[68:71], v[60:63]\n\t"
    "s_add_u32 m0, s85, 2880\n\t"
    "v_exp_f32 v53, v53\n\t"
    "v_mfma_f32_16x16x32_bf16 v[60:63], a[152:155], a[72:75], v[60:63]\n\t"
    "buffer_load_dwordx4 v4, s[80:83], s84 offen offset:192 lds\n\t"
    "v_cvt_pk_bf16_f32 v23, v25, v23\n\t"
    "v_mfma_f32_16x16x32_bf16 v[60:63], a[156:159], a[76:79], v[60:63]\n\t"
    "s_add_u32 s60, s60, 4096\n\t"
    "v_exp_f32 v54, v54\n\t"
    "v_smfmac_f32_16x16x64_bf16 a[236:239], a[248:251], v[40:47], v3\n\t"
    "s_add_u32 s61, s61, 4096\n\t"
    "ds_write_b128 v6, v[20:23] offset:1024\n\t"
    "v_mfma_f32_16x16x32_bf16 v[160:163], a[160:163], v[40:43], v[160:163]\n\t"
    "s_add_u32 s58, s58, 16384\n\t"
    "v_exp_f32 v55, v55\n\t"
    "v_mfma_f32_16x16x32_bf16 v[160:163], a[164:167], v[44:47], v[160:163]\n\t"
    "s_xor_b32 s59, s59, 0x4000\n\t"
    "ds_read_b128 v[20:23], v6 offset:3072\n\t"
    "v_cvt_pk_bf16_f32 v34, v52, v53\n\t"
    "v_mfma_f32_16x16x32_bf16 v[164:167], a[168:171], v[40:43], v[164:167]\n\t"
    "s_add_u32 s57, s57, 1\n\t"
    "v_cvt_pk_bf16_f32 v35, v54, v55\n\t"
    "s_waitcnt lgkmcnt(2)\n\t"
    "v_lshlrev_b32 v24, 16, v16\n\t"
    "v_mfma_f32_16x16x32_bf16 v[164:167], a[172:175], v[44:47], v[164:167]\n\t"
    "v_exp_f32 v56, v56\n\t"
    "v_mfma_f32_16x16x32_bf16 v[168:171], a[176:179], v[40:43], v[168:171]\n\t"
    "v_exp_f32 v57, v57\n\t"
    "v_mfma_f32_16x16x32_bf16 v[168:171], a[180:183], v[44:47], v[168:171]\n\t"
    "v_exp_f32 v58, v58\n\t"
    "v_mfma_f32_16x16x32_bf16 v[172:175], a[184:187], v[40:43], v[172:175]\n\t"
    "v_exp_f32 v59, v59\n\t"
    "v_mfma_f32_16x16x32_bf16 v[172:175], a[188:191], v[44:47], v[172:175]\n\t"
    "v_cvt_pk_bf16_f32 v36, v56, v57\n\t"
    "v_cvt_pk_bf16_f32 v37, v58, v59\n\t"
    "v_mfma_f32_16x16x32_bf16 v[176:179], a[192:195], v[40:43], v[176:179]\n\t"
    "v_exp_f32 v60, v60\n\t"
    "v_mfma_f32_16x16x32_bf16 v[176:179], a[196:199], v[44:47], v[176:179]\n\t"
    "v_and_b32 v16, s53, v16\n\t"
    "v_mul_f32 v24, s52, v24\n\t"
    "v_mfma_f32_16x16x32_bf16 v[180:183], a[200:203], v[40:43], v[180:183]\n\t"
    "v_exp_f32 v61, v61\n\t"
    "v_mfma_f32_16x16x32_bf16 v[180:183], a[204:207], v[44:47], v[180:183]\n\t"
    "v_exp_f32 v62, v62\n\t"
    "v_mfma_f32_16x16x32_bf16 v[184:187], a[208:211], v[40:43], v[184:187]\n\t"
    "v_exp_f32 v63, v63\n\t"
    "v_mfma_f32_16x16x32_bf16 v[184:187], a[212:215], v[44:47], v[184:187]\n\t"
    "v_cvt_pk_bf16_f32 v38, v60, v61\n\t"
    "v_cvt_pk_bf16_f32 v39, v62, v63\n\t"
    "v_mfma_f32_16x16x32_bf16 v[188:191], a[216:219], v[40:43], v[188:191]\n\t"
    "v_mul_f32 v16, s52, v16\n\t"
    "v_cvt_pk_bf16_f32 v16, v24, v16\n\t"
    "v_mfma_f32_16x16x32_bf16 v[188:191], a[220:223], v[44:47], v[188:191]\n\t"
    "v_lshlrev_b32 v25, 16, v17\n\t"
    "v_and_b32 v17, s53, v17\n\t"
    "v_mfma_f32_16x16x32_bf16 v[48:51], a[96:99], a[80:83], 0\n\t"
    "ds_read_b128 a[96:99], v0 offset:0\n\t"
    "v_mul_f32 v25, s52, v25\n\t"
    "v_mfma_f32_16x16x32_bf16 v[48:51], a[100:103], a[84:87], v[48:51]\n\t"
    "ds_read_b128 a[100:103], v0 offset:1024\n\t"
    "v_mul_f32 v17, s52, v17\n\t"
    "v_mfma_f32_16x16x32_bf16 v[48:51], a[104:107], a[88:91], v[48:51]\n\t"
    "ds_read_b128 a[104:107], v0 offset:2048\n\t"
    "v_cvt_pk_bf16_f32 v17, v25, v17\n\t"
    "v_mfma_f32_16x16x32_bf16 v[48:51], a[108:111], a[92:95], v[48:51]\n\t"
    "ds_read_b128 a[108:111], v0 offset:3072\n\t"
    "v_lshlrev_b32 v24, 16, v18\n\t"
    "v_mfma_f32_16x16x32_bf16 v[52:55], a[112:115], a[80:83], 0\n\t"
    "ds_read_b128 a[112:115], v0 offset:4096\n\t"
    "v_and_b32 v18, s53, v18\n\t"
    "v_mfma_f32_16x16x32_bf16 v[52:55], a[116:119], a[84:87], v[52:55]\n\t"
    "ds_read_b128 a[116:119], v0 offset:5120\n\t"
    "v_mul_f32 v24, s52, v24\n\t"
    "v_mfma_f32_16x16x32_bf16 v[52:55], a[120:123], a[88:91], v[52:55]\n\t"
    "v_exp_f32 v48, v48\n\t"
    "v_mfma_f32_16x16x32_bf16 v[52:55], a[124:127], a[92:95], v[52:55]\n\t"
    "v_exp_f32 v49, v49\n\t"
    "v_mfma_f32_16x16x32_bf16 v[56:59], a[128:131], a[80:83], 0\n\t"
    "v_exp_f32 v50, v50\n\t"
    "v_mfma_f32_16x16x32_bf16 v[56:59], a[132:135], a[84:87], v[56:59]\n\t"
    "v_exp_f32 v51, v51\n\t"
    "v_mfma_f32_16x16x32_bf16 v[56:59], a[136:139], a[88:91], v[56:59]\n\t"
    "ds_read_b128 a[120:123], v0 offset:6144\n\t"
    "v_cvt_pk_bf16_f32 v40, v48, v49\n\t"
    "v_mfma_f32_16x16x32_bf16 v[56:59], a[140:143], a[92:95], v[56:59]\n\t"
    "ds_read_b128 a[124:127], v0 offset:7168\n\t"
    "v_cvt_pk_bf16_f32 v41, v50, v51\n\t"
    "v_mfma_f32_16x16x32_bf16 v[60:63], a[144:147], a[80:83], 0\n\t"
    "v_exp_f32 v52, v52\n\t"
    "v_mfma_f32_16x16x32_bf16 v[60:63], a[148:151], a[84:87], v[60:63]\n\t"
    "v_exp_f32 v53, v53\n\t"
    "v_mfma_f32_16x16x32_bf16 v[60:63], a[152:155], a[88:91], v[60:63]\n\t"
    "v_exp_f32 v54, v54\n\t"
    "v_mfma_f32_16x16x32_bf16 v[60:63], a[156:159], a[92:95], v[60:63]\n\t"
    "v_exp_f32 v55, v55\n\t"
    "v_smfmac_f32_16x16x64_bf16 a[240:243], a[248:251], v[32:39], v3\n\t"
    "ds_read_b128 a[128:131], v0 offset:8192\n\t"
    "v_cvt_pk_bf16_f32 v42, v52, v53\n\t"
    "v_mfma_f32_16x16x32_bf16 v[192:195], a[160:163], v[32:35], v[192:195]\n\t"
    "ds_read_b128 a[132:135], v0 offset:9216\n\t"
    "v_mul_f32 v18, s52, v18\n\t"
    "v_mfma_f32_16x16x32_bf16 v[192:195], a[164:167], v[36:39], v[192:195]\n\t"
    "ds_read_b128 a[136:139], v0 offset:10240\n\t"
    "v_cvt_pk_bf16_f32 v43, v54, v55\n\t"
    "v_mfma_f32_16x16x32_bf16 v[196:199], a[168:171], v[32:35], v[196:199]\n\t"
    "v_exp_f32 v56, v56\n\t"
    "v_mfma_f32_16x16x32_bf16 v[196:199], a[172:175], v[36:39], v[196:199]\n\t"
    "v_exp_f32 v57, v57\n\t"
    "v_mfma_f32_16x16x32_bf16 v[200:203], a[176:179], v[32:35], v[200:203]\n\t"
    "ds_read_b128 a[140:143], v0 offset:11264\n\t"
    "v_cvt_pk_bf16_f32 v18, v24, v18\n\t"
    "v_mfma_f32_16x16x32_bf16 v[200:203], a[180:183], v[36:39], v[200:203]\n\t"
    "v_exp_f32 v58, v58\n\t"
    "v_mfma_f32_16x16x32_bf16 v[204:207], a[184:187], v[32:35], v[204:207]\n\t"
    "v_exp_f32 v59, v59\n\t"
    "v_mfma_f32_16x16x32_bf16 v[204:207], a[188:191], v[36:39], v[204:207]\n\t"
    "ds_read_b128 a[144:147], v0 offset:12288\n\t"
    "v_lshlrev_b32 v25, 16, v19\n\t"
    "v_mfma_f32_16x16x32_bf16 v[208:211], a[192:195], v[32:35], v[208:211]\n\t"
    "ds_read_b128 a[148:151], v0 offset:13312\n\t"
    "v_cvt_pk_bf16_f32 v44, v56, v57\n\t"
    "v_mfma_f32_16x16x32_bf16 v[208:211], a[196:199], v[36:39], v[208:211]\n\t"
    "ds_read_b128 a[152:155], v0 offset:14336\n\t"
    "v_cvt_pk_bf16_f32 v45, v58, v59\n\t"
    "v_mfma_f32_16x16x32_bf16 v[212:215], a[200:203], v[32:35], v[212:215]\n\t"
    "v_exp_f32 v60, v60\n\t"
    "v_mfma_f32_16x16x32_bf16 v[212:215], a[204:207], v[36:39], v[212:215]\n\t"
    "ds_read_b128 a[156:159], v0 offset:15360\n\t"
    "v_and_b32 v19, s53, v19\n\t"
    "v_mfma_f32_16x16x32_bf16 v[216:219], a[208:211], v[32:35], v[216:219]\n\t"
    "v_exp_f32 v61, v61\n\t"
    "v_mfma_f32_16x16x32_bf16 v[216:219], a[212:215], v[36:39], v[216:219]\n\t"
    "v_exp_f32 v62, v62\n\t"
    "v_mfma_f32_16x16x32_bf16 v[220:223], a[216:219], v[32:35], v[220:223]\n\t"
    "v_exp_f32 v63, v63\n\t"
    "v_mfma_f32_16x16x32_bf16 v[220:223], a[220:223], v[36:39], v[220:223]\n\t"
    "s_waitcnt lgkmcnt(15)\n\t"
    "v_mul_f32 v25, s52, v25\n\t"
    "v_cvt_pk_bf16_f32 v46, v60, v61\n\t"
    "v_mfma_f32_16x16x32_bf16 v[48:51], a[96:99], a[0:3], 0\n\t"
    "s_waitcnt lgkmcnt(14)\n\t"
    "v_cvt_pk_bf16_f32 v47, v62, v63\n\t"
    "v_mul_f32 v19, s52, v19\n\t"
    "v_mfma_f32_16x16x32_bf16 v[48:51], a[100:103], a[4:7], v[48:51]\n\t"
    "s_waitcnt lgkmcnt(13)\n\t"
    "v_cvt_pk_bf16_f32 v19, v25, v19\n\t"
    "v_xor_b32 v0, 0x4000, v0\n\t"
    "v_mfma_f32_16x16x32_bf16 v[48:51], a[104:107], a[8:11], v[48:51]\n\t"
    "s_waitcnt lgkmcnt(12)\n\t"
    "ds_write_b128 v6, v[16:19] offset:2048\n\t"
    "v_mfma_f32_16x16x32_bf16 v[48:51], a[108:111], a[12:15], v[48:51]\n\t"
    "s_waitcnt lgkmcnt(12)\n\t"
    "v_lshlrev_b32 v24, 16, v20\n\t"
    "v_and_b32 v20, s53, v20\n\t"
    "v_mfma_f32_16x16x32_bf16 v[52:55], a[112:115], a[0:3], 0\n\t"
    "s_waitcnt lgkmcnt(11)\n\t"
    "v_mul_f32 v24, s52, v24\n\t"
    "v_mul_f32 v20, s52, v20\n\t"
    "v_mfma_f32_16x16x32_bf16 v[52:55], a[116:119], a[4:7], v[52:55]\n\t"
    "s_waitcnt lgkmcnt(10)\n\t"
    "v_exp_f32 v48, v48\n\t"
    "v_mfma_f32_16x16x32_bf16 v[52:55], a[120:123], a[8:11], v[52:55]\n\t"
    "s_waitcnt lgkmcnt(9)\n\t"
    "v_exp_f32 v49, v49\n\t"
    "v_mfma_f32_16x16x32_bf16 v[52:55], a[124:127], a[12:15], v[52:55]\n\t"
    "s_waitcnt lgkmcnt(8)\n\t"
    "v_exp_f32 v50, v50\n\t"
    "v_mfma_f32_16x16x32_bf16 v[56:59], a[128:131], a[0:3], 0\n\t"
    "s_waitcnt lgkmcnt(7)\n\t"
    "v_exp_f32 v51, v51\n\t"
    "v_mfma_f32_16x16x32_bf16 v[56:59], a[132:135], a[4:7], v[56:59]\n\t"
    "s_waitcnt lgkmcnt(6)\n\t"
    "v_cvt_pk_bf16_f32 v32, v48, v49\n\t"
    "v_cvt_pk_bf16_f32 v33, v50, v51\n\t"
    "v_mfma_f32_16x16x32_bf16 v[56:59], a[136:139], a[8:11], v[56:59]\n\t"
    "s_waitcnt lgkmcnt(5)\n\t"
    "v_cvt_pk_bf16_f32 v20, v24, v20\n\t"
    "v_lshlrev_b32 v25, 16, v21\n\t"
    "v_mfma_f32_16x16x32_bf16 v[56:59], a[140:143], a[12:15], v[56:59]\n\t"
    "s_waitcnt lgkmcnt(4)\n\t"
    "v_exp_f32 v52, v52\n\t"
    "v_mfma_f32_16x16x32_bf16 v[60:63], a[144:147], a[0:3], 0\n\t"
    "s_waitcnt lgkmcnt(3)\n\t"
    "v_exp_f32 v53, v53\n\t"
    "v_mfma_f32_16x16x32_bf16 v[60:63], a[148:151], a[4:7], v[60:63]\n\t"
    "s_waitcnt lgkmcnt(2)\n\t"
    "v_exp_f32 v54, v54\n\t"
    "v_mfma_f32_16x16x32_bf16 v[60:63], a[152:155], a[8:11], v[60:63]\n\t"
    "s_waitcnt lgkmcnt(1)\n\t"
    "v_exp_f32 v55, v55\n\t"
    "v_mfma_f32_16x16x32_bf16 v[60:63], a[156:159], a[12:15], v[60:63]\n\t"
    "v_cvt_pk_bf16_f32 v34, v52, v53\n\t"
    "v_and_b32 v21, s53, v21\n\t"
    "v_smfmac_f32_16x16x64_bf16 a[244:247], a[248:251], v[40:47], v3\n\t"
    "v_cvt_pk_bf16_f32 v35, v54, v55\n\t"
    "v_mul_f32 v25, s52, v25\n\t"
    "v_mfma_f32_16x16x32_bf16 v[224:227], a[160:163], v[40:43], v[224:227]\n\t"
    "v_exp_f32 v56, v56\n\t"
    "v_mfma_f32_16x16x32_bf16 v[224:227], a[164:167], v[44:47], v[224:227]\n\t"
    "v_exp_f32 v57, v57\n\t"
    "v_mfma_f32_16x16x32_bf16 v[228:231], a[168:171], v[40:43], v[228:231]\n\t"
    "v_exp_f32 v58, v58\n\t"
    "v_mfma_f32_16x16x32_bf16 v[228:231], a[172:175], v[44:47], v[228:231]\n\t"
    "v_exp_f32 v59, v59\n\t"
    "v_mfma_f32_16x16x32_bf16 v[232:235], a[176:179], v[40:43], v[232:235]\n\t"
    "ds_read_b64_tr_b16 a[160:161], v1 offset:0\n\t"
    "v_mul_f32 v21, s52, v21\n\t"
    "v_mfma_f32_16x16x32_bf16 v[232:235], a[180:183], v[44:47], v[232:235]\n\t"
    "ds_read_b64_tr_b16 a[162:163], v1 offset:4096\n\t"
    "v_cvt_pk_bf16_f32 v36, v56, v57\n\t"
    "v_mfma_f32_16x16x32_bf16 v[236:239], a[184:187], v[40:43], v[236:239]\n\t"
    "ds_read_b64_tr_b16 a[164:165], v1 offset:8192\n\t"
    "v_cvt_pk_bf16_f32 v37, v58, v59\n\t"
    "v_mfma_f32_16x16x32_bf16 v[236:239], a[188:191], v[44:47], v[236:239]\n\t"
    "v_exp_f32 v60, v60\n\t"
    "v_mfma_f32_16x16x32_bf16 v[240:243], a[192:195], v[40:43], v[240:243]\n\t"
    "ds_read_b64_tr_b16 a[166:167], v1 offset:12288\n\t"
    "v_cvt_pk_bf16_f32 v21, v25, v21\n\t"
    "v_mfma_f32_16x16x32_bf16 v[240:243], a[196:199], v[44:47], v[240:243]\n\t"
    "v_exp_f32 v61, v61\n\t"
    "v_mfma_f32_16x16x32_bf16 v[244:247], a[200:203], v[40:43], v[244:247]\n\t"
    "v_exp_f32 v62, v62\n\t"
    "v_mfma_f32_16x16x32_bf16 v[244:247], a[204:207], v[44:47], v[244:247]\n\t"
    "v_exp_f32 v63, v63\n\t"
    "v_mfma_f32_16x16x32_bf16 v[248:251], a[208:211], v[40:43], v[248:251]\n\t"
    "ds_read_b64_tr_b16 a[168:169], v2 offset:0\n\t"
    "v_lshlrev_b32 v24, 16, v22\n\t"
    "v_mfma_f32_16x16x32_bf16 v[248:251], a[212:215], v[44:47], v[248:251]\n\t"
    "ds_read_b64_tr_b16 a[170:171], v2 offset:4096\n\t"
    "v_cvt_pk_bf16_f32 v38, v60, v61\n\t"
    "v_mfma_f32_16x16x32_bf16 v[252:255], a[216:219], v[40:43], v[252:255]\n\t"
    "ds_read_b64_tr_b16 a[172:173], v2 offset:8192\n\t"
    "v_cvt_pk_bf16_f32 v39, v62, v63\n\t"
    "v_mfma_f32_16x16x32_bf16 v[252:255], a[220:223], v[44:47], v[252:255]\n\t"
    "ds_read_b64_tr_b16 a[174:175], v2 offset:12288\n\t"
    "v_and_b32 v22, s53, v22\n\t"
    "v_mfma_f32_16x16x32_bf16 v[48:51], a[96:99], a[16:19], 0\n\t"
    "ds_read_b64_tr_b16 a[176:177], v1 offset:512\n\t"
    "ds_read_b64_tr_b16 a[178:179], v1 offset:4608\n\t"
    "v_mfma_f32_16x16x32_bf16 v[48:51], a[100:103], a[20:23], v[48:51]\n\t"
    "ds_read_b64_tr_b16 a[180:181], v1 offset:8704\n\t"
    "v_mul_f32 v24, s52, v24\n\t"
    "v_mfma_f32_16x16x32_bf16 v[48:51], a[104:107], a[24:27], v[48:51]\n\t"
    "ds_read_b64_tr_b16 a[182:183], v1 offset:12800\n\t"
    "ds_read_b64_tr_b16 a[184:185], v2 offset:512\n\t"
    "v_mfma_f32_16x16x32_bf16 v[48:51], a[108:111], a[28:31], v[48:51]\n\t"
    "ds_read_b64_tr_b16 a[186:187], v2 offset:4608\n\t"
    "v_mul_f32 v22, s52, v22\n\t"
    "v_mfma_f32_16x16x32_bf16 v[52:55], a[112:115], a[16:19], 0\n\t"
    "ds_read_b64_tr_b16 a[188:189], v2 offset:8704\n\t"
    "ds_read_b64_tr_b16 a[190:191], v2 offset:12800\n\t"
    "v_mfma_f32_16x16x32_bf16 v[52:55], a[116:119], a[20:23], v[52:55]\n\t"
    "ds_read_b64_tr_b16 a[192:193], v1 offset:1024\n\t"
    "ds_read_b64_tr_b16 a[194:195], v1 offset:5120\n\t"
    "v_mfma_f32_16x16x32_bf16 v[52:55], a[120:123], a[24:27], v[52:55]\n\t"
    "v_exp_f32 v48, v48\n\t"
    "v_mfma_f32_16x16x32_bf16 v[52:55], a[124:127], a[28:31], v[52:55]\n\t"
    "ds_read_b64_tr_b16 a[196:197], v1 offset:9216\n\t"
    "v_cvt_pk_bf16_f32 v22, v24, v22\n\t"
    "v_mfma_f32_16x16x32_bf16 v[56:59], a[128:131], a[16:19], 0\n\t"
    "v_exp_f32 v49, v49\n\t"
    "v_mfma_f32_16x16x32_bf16 v[56:59], a[132:135], a[20:23], v[56:59]\n\t"
    "ds_read_b64_tr_b16 a[198:199], v1 offset:13312\n\t"
    "ds_read_b64_tr_b16 a[200:201], v2 offset:1024\n\t"
    "v_mfma_f32_16x16x32_bf16 v[56:59], a[136:139], a[24:27], v[56:59]\n\t"
    "v_exp_f32 v50, v50\n\t"
    "v_mfma_f32_16x16x32_bf16 v[56:59], a[140:143], a[28:31], v[56:59]\n\t"
    "ds_read_b64_tr_b16 a[202:203], v2 offset:5120\n\t"
    "ds_read_b64_tr_b16 a[204:205], v2 offset:9216\n\t"
    "v_mfma_f32_16x16x32_bf16 v[60:63], a[144:147], a[16:19], 0\n\t"
    "v_exp_f32 v51, v51\n\t"
    "v_mfma_f32_16x16x32_bf16 v[60:63], a[148:151], a[20:23], v[60:63]\n\t"
    "ds_read_b64_tr_b16 a[206:207], v2 offset:13312\n\t"
    "v_lshlrev_b32 v25, 16, v23\n\t"
    "v_mfma_f32_16x16x32_bf16 v[60:63], a[152:155], a[24:27], v[60:63]\n\t"
    "ds_read_b64_tr_b16 a[208:209], v1 offset:1536\n\t"
    "v_cvt_pk_bf16_f32 v40, v48, v49\n\t"
    "v_mfma_f32_16x16x32_bf16 v[60:63], a[156:159], a[28:31], v[60:63]\n\t"
    "ds_read_b64_tr_b16 a[210:211], v1 offset:5632\n\t"
    "v_cvt_pk_bf16_f32 v41, v50, v51\n\t"
    "v_smfmac_f32_16x16x64_bf16 a[224:227], a[248:251], v[32:39], v3\n\t"
    "s_waitcnt lgkmcnt(15)\n\t"
    "ds_read_b64_tr_b16 a[212:213], v1 offset:9728\n\t"
    "v_and_b32 v23, s53, v23\n\t"
    "v_mfma_f32_16x16x32_bf16 v[64:67], a[160:163], v[32:35], v[64:67]\n\t"
    "v_exp_f32 v52, v52\n\t"
    "v_mfma_f32_16x16x32_bf16 v[64:67], a[164:167], v[36:39], v[64:67]\n\t"
    "ds_read_b64_tr_b16 a[214:215], v1 offset:13824\n\t"
    "ds_read_b64_tr_b16 a[216:217], v2 offset:1536\n\t"
    "v_mfma_f32_16x16x32_bf16 v[68:71], a[168:171], v[32:35], v[68:71]\n\t"
    "v_exp_f32 v53, v53\n\t"
    "v_mfma_f32_16x16x32_bf16 v[68:71], a[172:175], v[36:39], v[68:71]\n\t"
    "ds_read_b64_tr_b16 a[218:219], v2 offset:5632\n\t"
    "ds_read_b64_tr_b16 a[220:221], v2 offset:9728\n\t"
    "v_mfma_f32_16x16x32_bf16 v[72:75], a[176:179], v[32:35], v[72:75]\n\t"
    "s_waitcnt lgkmcnt(15)\n\t"
    "ds_read_b64_tr_b16 a[222:223], v2 offset:13824\n\t"
    "v_mul_f32 v25, s52, v25\n\t"
    "v_mfma_f32_16x16x32_bf16 v[72:75], a[180:183], v[36:39], v[72:75]\n\t"
    "v_exp_f32 v54, v54\n\t"
    "v_mfma_f32_16x16x32_bf16 v[76:79], a[184:187], v[32:35], v[76:79]\n\t"
    "v_mul_f32 v23, s52, v23\n\t"
    "v_cvt_pk_bf16_f32 v23, v25, v23\n\t"
    "v_mfma_f32_16x16x32_bf16 v[76:79], a[188:191], v[36:39], v[76:79]\n\t"
    "s_waitcnt lgkmcnt(14)\n\t"
    "v_exp_f32 v55, v55\n\t"
    "v_mfma_f32_16x16x32_bf16 v[80:83], a[192:195], v[32:35], v[80:83]\n\t"
    "s_waitcnt lgkmcnt(12)\n\t"
    "ds_write_b128 v6, v[20:23] offset:3072\n\t"
    "v_mfma_f32_16x16x32_bf16 v[80:83], a[196:199], v[36:39], v[80:83]\n\t"
    "s_waitcnt lgkmcnt(11)\n\t"
    "v_cvt_pk_bf16_f32 v42, v52, v53\n\t"
    "v_cvt_pk_bf16_f32 v43, v54, v55\n\t"
    "v_mfma_f32_16x16x32_bf16 v[84:87], a[200:203], v[32:35], v[84:87]\n\t"
    "s_waitcnt lgkmcnt(9)\n\t"
    "v_exp_f32 v56, v56\n\t"
    "v_mfma_f32_16x16x32_bf16 v[84:87], a[204:207], v[36:39], v[84:87]\n\t"
    "s_waitcnt lgkmcnt(7)\n\t"
    "v_exp_f32 v57, v57\n\t"
    "v_mfma_f32_16x16x32_bf16 v[88:91], a[208:211], v[32:35], v[88:91]\n\t"
    "s_waitcnt lgkmcnt(5)\n\t"
    "v_exp_f32 v58, v58\n\t"
    "v_mfma_f32_16x16x32_bf16 v[88:91], a[212:215], v[36:39], v[88:91]\n\t"
    "s_waitcnt lgkmcnt(3)\n\t"
    "v_exp_f32 v59, v59\n\t"
    "v_mfma_f32_16x16x32_bf16 v[92:95], a[216:219], v[32:35], v[92:95]\n\t"
    "s_waitcnt lgkmcnt(1)\n\t"
    "v_add_u32 v6, 0x1000, v6\n\t"
    "v_cvt_pk_bf16_f32 v44, v56, v57\n\t"
    "v_mfma_f32_16x16x32_bf16 v[92:95], a[220:223], v[36:39], v[92:95]\n\t"
    "v_xor_b32 v1, 0x4000, v1\n\t"
    "v_cvt_pk_bf16_f32 v45, v58, v59\n\t"
    "v_xor_b32 v2, 0x4000, v2\n\t"
    "s_cmp_lg_u32 s57, 7\n\t"
    "s_cbranch_scc1 L_L0\n\t"
    "v_mfma_f32_16x16x32_bf16 v[48:51], a[96:99], a[32:35], 0\n\t"
    "s_waitcnt vmcnt(0) lgkmcnt(0)\n\t"
    "s_barrier\n\t"
    "v_subrev_u32 v6, 0x6000, v6\n\t"
    "v_accvgpr_read_b32 v14, a224\n\t"
    "v_mfma_f32_16x16x32_bf16 v[48:51], a[100:103], a[36:39], v[48:51]\n\t"
    "s_lshl_b32 s58, s56, 12\n\t"
    "v_exp_f32 v60, v60\n\t"
    "v_mfma_f32_16x16x32_bf16 v[48:51], a[104:107], a[40:43], v[48:51]\n\t"
    "s_add_u32 s58, s58, 81920\n\t"
    "v_exp_f32 v61, v61\n\t"
    "v_mfma_f32_16x16x32_bf16 v[48:51], a[108:111], a[44:47], v[48:51]\n\t"
    "s_mul_i32 s62, s56, 24576\n\t"
    "v_exp_f32 v62, v62\n\t"
    "v_mfma_f32_16x16x32_bf16 v[52:55], a[112:115], a[32:35], 0\n\t"
    "s_add_u32 s64, s62, 20480\n\t"
    "v_exp_f32 v63, v63\n\t"
    "v_mfma_f32_16x16x32_bf16 v[52:55], a[116:119], a[36:39], v[52:55]\n\t"
    "s_add_u32 m0, s59, 0\n\t"
    "ds_read_b128 a[0:3], v6 offset:0\n\t"
    "v_cvt_pk_bf16_f32 v46, v60, v61\n\t"
    "v_mfma_f32_16x16x32_bf16 v[52:55], a[120:123], a[40:43], v[52:55]\n\t"
    "ds_read_b128 a[4:7], v6 offset:1024\n\t"
    "v_cvt_pk_bf16_f32 v47, v62, v63\n\t"
    "v_mfma_f32_16x16x32_bf16 v[52:55], a[124:127], a[44:47], v[52:55]\n\t"
    "v_exp_f32 v48, v48\n\t"
    "v_mfma_f32_16x16x32_bf16 v[56:59], a[128:131], a[32:35], 0\n\t"
    "v_exp_f32 v49, v49\n\t"
    "v_mfma_f32_16x16x32_bf16 v[56:59], a[132:135], a[36:39], v[56:59]\n\t"
    "ds_read_b128 a[8:11], v6 offset:2048\n\t"
    "ds_read_b128 a[12:15], v6 offset:3072\n\t"
    "v_mfma_f32_16x16x32_bf16 v[56:59], a[136:139], a[40:43], v[56:59]\n\t"
    "v_exp_f32 v50, v50\n\t"
    "v_mfma_f32_16x16x32_bf16 v[56:59], a[140:143], a[44:47], v[56:59]\n\t"
    "buffer_load_dwordx4 v4, s[36:39], s58 offen offset:0 lds\n\t"
    "ds_read_b128 a[16:19], v6 offset:4096\n\t"
    "v_mfma_f32_16x16x32_bf16 v[60:63], a[144:147], a[32:35], 0\n\t"
    "v_exp_f32 v51, v51\n\t"
    "v_mfma_f32_16x16x32_bf16 v[60:63], a[148:151], a[36:39], v[60:63]\n\t"
    "ds_read_b128 a[20:23], v6 offset:5120\n\t"
    "ds_read_b128 a[24:27], v6 offset:6144\n\t"
    "v_mfma_f32_16x16x32_bf16 v[60:63], a[152:155], a[40:43], v[60:63]\n\t"
    "ds_read_b128 a[28:31], v6 offset:7168\n\t"
    "v_cvt_pk_bf16_f32 v32, v48, v49\n\t"
    "v_mfma_f32_16x16x32_bf16 v[60:63], a[156:159], a[44:47], v[60:63]\n\t"
    "s_add_u32 m0, s59, 960\n\t"
    "ds_read_b128 a[32:35], v6 offset:8192\n\t"
    "v_cvt_pk_bf16_f32 v33, v50, v51\n\t"
    "v_smfmac_f32_16x16x64_bf16 a[228:231], a[248:251], v[40:47], v3\n\t"
    "v_exp_f32 v52, v52\n\t"
    "v_mfma_f32_16x16x32_bf16 v[96:99], a[160:163], v[40:43], v[96:99]\n\t"
    "v_exp_f32 v53, v53\n\t"
    "v_mfma_f32_16x16x32_bf16 v[96:99], a[164:167], v[44:47], v[96:99]\n\t"
    "v_exp_f32 v54, v54\n\t"
    "v_mfma_f32_16x16x32_bf16 v[100:103], a[168:171], v[40:43], v[100:103]\n\t"
    "v_exp_f32 v55, v55\n\t"
    "v_mfma_f32_16x16x32_bf16 v[100:103], a[172:175], v[44:47], v[100:103]\n\t"
    "buffer_load_dwordx4 v4, s[36:39], s58 offen offset:64 lds\n\t"
    "v_cvt_pk_bf16_f32 v34, v52, v53\n\t"
    "v_mfma_f32_16x16x32_bf16 v[104:107], a[176:179], v[40:43], v[104:107]\n\t"
    "ds_read_b128 a[36:39], v6 offset:9216\n\t"
    "v_cvt_pk_bf16_f32 v35, v54, v55\n\t"
    "v_mfma_f32_16x16x32_bf16 v[104:107], a[180:183], v[44:47], v[104:107]\n\t"
    "v_exp_f32 v56, v56\n\t"
    "v_mfma_f32_16x16x32_bf16 v[108:111], a[184:187], v[40:43], v[108:111]\n\t"
    "v_exp_f32 v57, v57\n\t"
    "v_mfma_f32_16x16x32_bf16 v[108:111], a[188:191], v[44:47], v[108:111]\n\t"
    "v_exp_f32 v58, v58\n\t"
    "v_mfma_f32_16x16x32_bf16 v[112:115], a[192:195], v[40:43], v[112:115]\n\t"
    "s_add_u32 m0, s59, 1920\n\t"
    "v_exp_f32 v59, v59\n\t"
    "v_mfma_f32_16x16x32_bf16 v[112:115], a[196:199], v[44:47], v[112:115]\n\t"
    "ds_read_b128 a[40:43], v6 offset:10240\n\t"
    "v_cvt_pk_bf16_f32 v36, v56, v57\n\t"
    "v_mfma_f32_16x16x32_bf16 v[116:119], a[200:203], v[40:43], v[116:119]\n\t"
    "ds_read_b128 a[44:47], v6 offset:11264\n\t"
    "v_cvt_pk_bf16_f32 v37, v58, v59\n\t"
    "v_mfma_f32_16x16x32_bf16 v[116:119], a[204:207], v[44:47], v[116:119]\n\t"
    "v_exp_f32 v60, v60\n\t"
    "v_mfma_f32_16x16x32_bf16 v[120:123], a[208:211], v[40:43], v[120:123]\n\t"
    "v_exp_f32 v61, v61\n\t"
    "v_mfma_f32_16x16x32_bf16 v[120:123], a[212:215], v[44:47], v[120:123]\n\t"
    "v_exp_f32 v62, v62\n\t"
    "v_mfma_f32_16x16x32_bf16 v[124:127], a[216:219], v[40:43], v[124:127]\n\t"
    "v_exp_f32 v63, v63\n\t"
    "v_mfma_f32_16x16x32_bf16 v[124:127], a[220:223], v[44:47], v[124:127]\n\t"
    "buffer_load_dwordx4 v4, s[36:39], s58 offen offset:128 lds\n\t"
    "v_cvt_pk_bf16_f32 v38, v60, v61\n\t"
    "v_mfma_f32_16x16x32_bf16 v[48:51], a[96:99], a[48:51], 0\n\t"
    "v_cvt_pk_bf16_f32 v39, v62, v63\n\t"
    "v_accvgpr_read_b32 v15, a225\n\t"
    "v_mfma_f32_16x16x32_bf16 v[48:51], a[100:103], a[52:55], v[48:51]\n\t"
    "v_add_f32 v11, v14, v15\n\t"
    "v_subrev_u32 v14, s54, v11\n\t"
    "v_mfma_f32_16x16x32_bf16 v[48:51], a[104:107], a[56:59], v[48:51]\n\t"
    "s_add_u32 m0, s59, 2880\n\t"
    "v_max_u32 v10, v10, v14\n\t"
    "v_accvgpr_read_b32 v14, a228\n\t"
    "v_mfma_f32_16x16x32_bf16 v[48:51], a[108:111], a[60:63], v[48:51]\n\t"
    "buffer_load_dwordx4 v4, s[36:39], s58 offen offset:192 lds\n\t"
    "v_accvgpr_read_b32 v15, a229\n\t"
    "v_mfma_f32_16x16x32_bf16 v[52:55], a[112:115], a[48:51], 0\n\t"
    "v_rcp_f32 v11, v11\n\t"
    "v_mfma_f32_16x16x32_bf16 v[52:55], a[116:119], a[52:55], v[52:55]\n\t"
    "v_accvgpr_write_b32 a224, 0\n\t"
    "v_accvgpr_write_b32 a225, 0\n\t"
    "v_mfma_f32_16x16x32_bf16 v[52:55], a[120:123], a[56:59], v[52:55]\n\t"
    "v_exp_f32 v48, v48\n\t"
    "v_mfma_f32_16x16x32_bf16 v[52:55], a[124:127], a[60:63], v[52:55]\n\t"
    "v_exp_f32 v49, v49\n\t"
    "v_mfma_f32_16x16x32_bf16 v[56:59], a[128:131], a[48:51], 0\n\t"
    "v_exp_f32 v50, v50\n\t"
    "v_mfma_f32_16x16x32_bf16 v[56:59], a[132:135], a[52:55], v[56:59]\n\t"
    "v_exp_f32 v51, v51\n\t"
    "v_mfma_f32_16x16x32_bf16 v[56:59], a[136:139], a[56:59], v[56:59]\n\t"
    "v_cvt_pk_bf16_f32 v40, v48, v49\n\t"
    "v_cvt_pk_bf16_f32 v41, v50, v51\n\t"
    "v_mfma_f32_16x16x32_bf16 v[56:59], a[140:143], a[60:63], v[56:59]\n\t"
    "v_exp_f32 v52, v52\n\t"
    "v_mfma_f32_16x16x32_bf16 v[60:63], a[144:147], a[48:51], 0\n\t"
    "s_add_u32 m0, s59, 32768\n\t"
    "v_exp_f32 v53, v53\n\t"
    "v_mfma_f32_16x16x32_bf16 v[60:63], a[148:151], a[52:55], v[60:63]\n\t"
    "v_exp_f32 v54, v54\n\t"
    "v_mfma_f32_16x16x32_bf16 v[60:63], a[152:155], a[56:59], v[60:63]\n\t"
    "v_exp_f32 v55, v55\n\t"
    "v_mfma_f32_16x16x32_bf16 v[60:63], a[156:159], a[60:63], v[60:63]\n\t"
    "buffer_load_dwordx4 v5, s[40:43], s58 offen offset:0 lds\n\t"
    "v_cvt_pk_bf16_f32 v42, v52, v53\n\t"
    "v_smfmac_f32_16x16x64_bf16 a[232:235], a[248:251], v[32:39], v3\n\t"
    "ds_read_b128 a[48:51], v6 offset:12288\n\t"
    "v_cvt_pk_bf16_f32 v43, v54, v55\n\t"
    "v_mfma_f32_16x16x32_bf16 v[128:131], a[160:163], v[32:35], v[128:131]\n\t"
    "v_exp_f32 v56, v56\n\t"
    "v_mfma_f32_16x16x32_bf16 v[128:131], a[164:167], v[36:39], v[128:131]\n\t"
    "v_exp_f32 v57, v57\n\t"
    "v_mfma_f32_16x16x32_bf16 v[132:135], a[168:171], v[32:35], v[132:135]\n\t"
    "v_exp_f32 v58, v58\n\t"
    "v_mfma_f32_16x16x32_bf16 v[132:135], a[172:175], v[36:39], v[132:135]\n\t"
    "v_exp_f32 v59, v59\n\t"
    "v_mfma_f32_16x16x32_bf16 v[136:139], a[176:179], v[32:35], v[136:139]\n\t"
    "ds_read_b128 a[52:55], v6 offset:13312\n\t"
    "v_cvt_pk_bf16_f32 v44, v56, v57\n\t"
    "v_mfma_f32_16x16x32_bf16 v[136:139], a[180:183], v[36:39], v[136:139]\n\t"
    "ds_read_b128 a[56:59], v6 offset:14336\n\t"
    "v_cvt_pk_bf16_f32 v45, v58, v59\n\t"
    "v_mfma_f32_16x16x32_bf16 v[140:143], a[184:187], v[32:35], v[140:143]\n\t"
    "v_exp_f32 v60, v60\n\t"
    "v_mfma_f32_16x16x32_bf16 v[140:143], a[188:191], v[36:39], v[140:143]\n\t"
    "v_exp_f32 v61, v61\n\t"
    "v_mfma_f32_16x16x32_bf16 v[144:147], a[192:195], v[32:35], v[144:147]\n\t"
    "v_exp_f32 v62, v62\n\t"
    "v_mfma_f32_16x16x32_bf16 v[144:147], a[196:199], v[36:39], v[144:147]\n\t"
    "v_exp_f32 v63, v63\n\t"
    "v_mfma_f32_16x16x32_bf16 v[148:151], a[200:203], v[32:35], v[148:151]\n\t"
    "ds_read_b128 a[60:63], v6 offset:15360\n\t"
    "v_cvt_pk_bf16_f32 v46, v60, v61\n\t"
    "v_mfma_f32_16x16x32_bf16 v[148:151], a[204:207], v[36:39], v[148:151]\n\t"
    "buffer_load_dwordx4 v5, s[40:43], s58 offen offset:2048 lds\n\t"
    "v_cvt_pk_bf16_f32 v47, v62, v63\n\t"
    "v_mfma_f32_16x16x32_bf16 v[152:155], a[208:211], v[32:35], v[152:155]\n\t"
    "v_mul_f32 v64, v11, v64\n\t"
    "v_mul_f32 v65, v11, v65\n\t"
    "v_mfma_f32_16x16x32_bf16 v[152:155], a[212:215], v[36:39], v[152:155]\n\t"
    "v_mul_f32 v66, v11, v66\n\t"
    "v_mul_f32 v67, v11, v67\n\t"
    "v_mfma_f32_16x16x32_bf16 v[156:159], a[216:219], v[32:35], v[156:159]\n\t"
    "v_cvt_pk_bf16_f32 v64, v64, v65\n\t"
    "v_cvt_pk_bf16_f32 v65, v66, v67\n\t"
    "v_mfma_f32_16x16x32_bf16 v[156:159], a[220:223], v[36:39], v[156:159]\n\t"
    "s_add_u32 m0, s59, 33664\n\t"
    "ds_write_b64 v7, v[64:65] offset:0\n\t"
    "v_mfma_f32_16x16x32_bf16 v[48:51], a[96:99], a[64:67], 0\n\t"
    "buffer_load_dwordx4 v5, s[40:43], s58 offen offset:128 lds\n\t"
    "v_mul_f32 v68, v11, v68\n\t"
    "v_mfma_f32_16x16x32_bf16 v[48:51], a[100:103], a[68:71], v[48:51]\n\t"
    "v_mul_f32 v69, v11, v69\n\t"
    "v_mul_f32 v70, v11, v70\n\t"
    "v_mfma_f32_16x16x32_bf16 v[48:51], a[104:107], a[72:75], v[48:51]\n\t"
    "v_mul_f32 v71, v11, v71\n\t"
    "v_cvt_pk_bf16_f32 v68, v68, v69\n\t"
    "v_mfma_f32_16x16x32_bf16 v[48:51], a[108:111], a[76:79], v[48:51]\n\t"
    "v_cvt_pk_bf16_f32 v69, v70, v71\n\t"
    "v_add_f32 v12, v14, v15\n\t"
    "v_mfma_f32_16x16x32_bf16 v[52:55], a[112:115], a[64:67], 0\n\t"
    "ds_write_b64 v7, v[68:69] offset:32\n\t"
    "v_mfma_f32_16x16x32_bf16 v[52:55], a[116:119], a[68:71], v[52:55]\n\t"
    "v_mul_f32 v72, v11, v72\n\t"
    "v_mul_f32 v73, v11, v73\n\t"
    "v_mfma_f32_16x16x32_bf16 v[52:55], a[120:123], a[72:75], v[52:55]\n\t"
    "v_exp_f32 v48, v48\n\t"
    "v_mfma_f32_16x16x32_bf16 v[52:55], a[124:127], a[76:79], v[52:55]\n\t"
    "v_exp_f32 v49, v49\n\t"
    "v_mfma_f32_16x16x32_bf16 v[56:59], a[128:131], a[64:67], 0\n\t"
    "v_exp_f32 v50, v50\n\t"
    "v_mfma_f32_16x16x32_bf16 v[56:59], a[132:135], a[68:71], v[56:59]\n\t"
    "v_exp_f32 v51, v51\n\t"
    "v_mfma_f32_16x16x32_bf16 v[56:59], a[136:139], a[72:75], v[56:59]\n\t"
    "buffer_load_dwordx4 v5, s[40:43], s58 offen offset:2176 lds\n\t"
    "v_cvt_pk_bf16_f32 v32, v48, v49\n\t"
    "v_mfma_f32_16x16x32_bf16 v[56:59], a[140:143], a[76:79], v[56:59]\n\t"
    "s_xor_b32 s59, s59, 0x4000\n\t"
    "v_cvt_pk_bf16_f32 v33, v50, v51\n\t"
    "v_mul_f32 v74, v11, v74\n\t"
    "v_mfma_f32_16x16x32_bf16 v[60:63], a[144:147], a[64:67], 0\n\t"
    "s_add_u32 s58, s58, 4294950912\n\t"
    "v_exp_f32 v52, v52\n\t"
    "v_mfma_f32_16x16x32_bf16 v[60:63], a[148:151], a[68:71], v[60:63]\n\t"
    "s_mov_b32 s57, 6\n\t"
    "v_exp_f32 v53, v53\n\t"
    "v_mfma_f32_16x16x32_bf16 v[60:63], a[152:155], a[72:75], v[60:63]\n\t"
    "v_exp_f32 v54, v54\n\t"
    "v_mfma_f32_16x16x32_bf16 v[60:63], a[156:159], a[76:79], v[60:63]\n\t"
    "v_exp_f32 v55, v55\n\t"
    "v_smfmac_f32_16x16x64_bf16 a[236:239], a[248:251], v[40:47], v3\n\t"
    "ds_read_b128 a[64:67], v6 offset:16384\n\t"
    "v_cvt_pk_bf16_f32 v34, v52, v53\n\t"
    "v_mfma_f32_16x16x32_bf16 v[160:163], a[160:163], v[40:43], v[160:163]\n\t"
    "ds_read_b128 a[68:71], v6 offset:17408\n\t"
    "v_cvt_pk_bf16_f32 v35, v54, v55\n\t"
    "v_mfma_f32_16x16x32_bf16 v[160:163], a[164:167], v[44:47], v[160:163]\n\t"
    "v_exp_f32 v56, v56\n\t"
    "v_mfma_f32_16x16x32_bf16 v[164:167], a[168:171], v[40:43], v[164:167]\n\t"
    "v_exp_f32 v57, v57\n\t"
    "v_mfma_f32_16x16x32_bf16 v[164:167], a[172:175], v[44:47], v[164:167]\n\t"
    "v_exp_f32 v58, v58\n\t"
    "v_mfma_f32_16x16x32_bf16 v[168:171], a[176:179], v[40:43], v[168:171]\n\t"
    "v_exp_f32 v59, v59\n\t"
    "v_mfma_f32_16x16x32_bf16 v[168:171], a[180:183], v[44:47], v[168:171]\n\t"
    "ds_read_b128 a[72:75], v6 offset:18432\n\t"
    "v_cvt_pk_bf16_f32 v36, v56, v57\n\t"
    "v_mfma_f32_16x16x32_bf16 v[172:175], a[184:187], v[40:43], v[172:175]\n\t"
    "ds_read_b128 a[76:79], v6 offset:19456\n\t"
    "v_cvt_pk_bf16_f32 v37, v58, v59\n\t"
    "v_mfma_f32_16x16x32_bf16 v[172:175], a[188:191], v[44:47], v[172:175]\n\t"
    "v_exp_f32 v60, v60\n\t"
    "v_mfma_f32_16x16x32_bf16 v[176:179], a[192:195], v[40:43], v[176:179]\n\t"
    "v_exp_f32 v61, v61\n\t"
    "v_mfma_f32_16x16x32_bf16 v[176:179], a[196:199], v[44:47], v[176:179]\n\t"
    "v_exp_f32 v62, v62\n\t"
    "v_mfma_f32_16x16x32_bf16 v[180:183], a[200:203], v[40:43], v[180:183]\n\t"
    "v_exp_f32 v63, v63\n\t"
    "v_mfma_f32_16x16x32_bf16 v[180:183], a[204:207], v[44:47], v[180:183]\n\t"
    "v_cvt_pk_bf16_f32 v38, v60, v61\n\t"
    "v_cvt_pk_bf16_f32 v39, v62, v63\n\t"
    "v_mfma_f32_16x16x32_bf16 v[184:187], a[208:211], v[40:43], v[184:187]\n\t"
    "v_mul_f32 v75, v11, v75\n\t"
    "v_cvt_pk_bf16_f32 v72, v72, v73\n\t"
    "v_mfma_f32_16x16x32_bf16 v[184:187], a[212:215], v[44:47], v[184:187]\n\t"
    "v_cvt_pk_bf16_f32 v73, v74, v75\n\t"
    "v_subrev_u32 v14, s54, v12\n\t"
    "v_mfma_f32_16x16x32_bf16 v[188:191], a[216:219], v[40:43], v[188:191]\n\t"
    "ds_write_b64 v7, v[72:73] offset:64\n\t"
    "v_mfma_f32_16x16x32_bf16 v[188:191], a[220:223], v[44:47], v[188:191]\n\t"
    "v_mul_f32 v76, v11, v76\n\t"
    "v_mul_f32 v77, v11, v77\n\t"
    "v_mfma_f32_16x16x32_bf16 v[48:51], a[96:99], a[80:83], 0\n\t"
    "v_mul_f32 v78, v11, v78\n\t"
    "v_mul_f32 v79, v11, v79\n\t"
    "v_mfma_f32_16x16x32_bf16 v[48:51], a[100:103], a[84:87], v[48:51]\n\t"
    "v_cvt_pk_bf16_f32 v76, v76, v77\n\t"
    "v_max_u32 v10, v10, v14\n\t"
    "v_mfma_f32_16x16x32_bf16 v[48:51], a[104:107], a[88:91], v[48:51]\n\t"
    "v_cvt_pk_bf16_f32 v77, v78, v79\n\t"
    "v_accvgpr_read_b32 v14, a232\n\t"
    "v_mfma_f32_16x16x32_bf16 v[48:51], a[108:111], a[92:95], v[48:51]\n\t"
    "v_rcp_f32 v12, v12\n\t"
    "v_mfma_f32_16x16x32_bf16 v[52:55], a[112:115], a[80:83], 0\n\t"
    "ds_write_b64 v7, v[76:77] offset:96\n\t"
    "v_mfma_f32_16x16x32_bf16 v[52:55], a[116:119], a[84:87], v[52:55]\n\t"
    "v_accvgpr_write_b32 a228, 0\n\t"
    "v_mul_f32 v80, v11, v80\n\t"
    "v_mfma_f32_16x16x32_bf16 v[52:55], a[120:123], a[88:91], v[52:55]\n\t"
    "v_accvgpr_write_b32 a229, 0\n\t"
    "v_mul_f32 v81, v11, v81\n\t"
    "v_mfma_f32_16x16x32_bf16 v[52:55], a[124:127], a[92:95], v[52:55]\n\t"
    "v_exp_f32 v48, v48\n\t"
    "v_mfma_f32_16x16x32_bf16 v[56:59], a[128:131], a[80:83], 0\n\t"
    "v_exp_f32 v49, v49\n\t"
    "v_mfma_f32_16x16x32_bf16 v[56:59], a[132:135], a[84:87], v[56:59]\n\t"
    "v_exp_f32 v50, v50\n\t"
    "v_mfma_f32_16x16x32_bf16 v[56:59], a[136:139], a[88:91], v[56:59]\n\t"
    "v_exp_f32 v51, v51\n\t"
    "v_mfma_f32_16x16x32_bf16 v[56:59], a[140:143], a[92:95], v[56:59]\n\t"
    "v_cvt_pk_bf16_f32 v40, v48, v49\n\t"
    "v_cvt_pk_bf16_f32 v41, v50, v51\n\t"
    "v_mfma_f32_16x16x32_bf16 v[60:63], a[144:147], a[80:83], 0\n\t"
    "v_exp_f32 v52, v52\n\t"
    "v_mfma_f32_16x16x32_bf16 v[60:63], a[148:151], a[84:87], v[60:63]\n\t"
    "v_exp_f32 v53, v53\n\t"
    "v_mfma_f32_16x16x32_bf16 v[60:63], a[152:155], a[88:91], v[60:63]\n\t"
    "v_exp_f32 v54, v54\n\t"
    "v_mfma_f32_16x16x32_bf16 v[60:63], a[156:159], a[92:95], v[60:63]\n\t"
    "v_exp_f32 v55, v55\n\t"
    "v_smfmac_f32_16x16x64_bf16 a[240:243], a[248:251], v[32:39], v3\n\t"
    "ds_read_b128 a[80:83], v6 offset:20480\n\t"
    "v_cvt_pk_bf16_f32 v42, v52, v53\n\t"
    "v_mfma_f32_16x16x32_bf16 v[192:195], a[160:163], v[32:35], v[192:195]\n\t"
    "ds_read_b128 a[84:87], v6 offset:21504\n\t"
    "v_cvt_pk_bf16_f32 v43, v54, v55\n\t"
    "v_mfma_f32_16x16x32_bf16 v[192:195], a[164:167], v[36:39], v[192:195]\n\t"
    "v_exp_f32 v56, v56\n\t"
    "v_mfma_f32_16x16x32_bf16 v[196:199], a[168:171], v[32:35], v[196:199]\n\t"
    "v_exp_f32 v57, v57\n\t"
    "v_mfma_f32_16x16x32_bf16 v[196:199], a[172:175], v[36:39], v[196:199]\n\t"
    "v_exp_f32 v58, v58\n\t"
    "v_mfma_f32_16x16x32_bf16 v[200:203], a[176:179], v[32:35], v[200:203]\n\t"
    "v_exp_f32 v59, v59\n\t"
    "v_mfma_f32_16x16x32_bf16 v[200:203], a[180:183], v[36:39], v[200:203]\n\t"
    "ds_read_b128 a[88:91], v6 offset:22528\n\t"
    "v_cvt_pk_bf16_f32 v44, v56, v57\n\t"
    "v_mfma_f32_16x16x32_bf16 v[204:207], a[184:187], v[32:35], v[204:207]\n\t"
    "ds_read_b128 a[92:95], v6 offset:23552\n\t"
    "v_cvt_pk_bf16_f32 v45, v58, v59\n\t"
    "v_mfma_f32_16x16x32_bf16 v[204:207], a[188:191], v[36:39], v[204:207]\n\t"
    "v_exp_f32 v60, v60\n\t"
    "v_mfma_f32_16x16x32_bf16 v[208:211], a[192:195], v[32:35], v[208:211]\n\t"
    "v_exp_f32 v61, v61\n\t"
    "v_mfma_f32_16x16x32_bf16 v[208:211], a[196:199], v[36:39], v[208:211]\n\t"
    "v_exp_f32 v62, v62\n\t"
    "v_mfma_f32_16x16x32_bf16 v[212:215], a[200:203], v[32:35], v[212:215]\n\t"
    "v_exp_f32 v63, v63\n\t"
    "v_mfma_f32_16x16x32_bf16 v[212:215], a[204:207], v[36:39], v[212:215]\n\t"
    "v_cvt_pk_bf16_f32 v46, v60, v61\n\t"
    "v_cvt_pk_bf16_f32 v47, v62, v63\n\t"
    "v_mfma_f32_16x16x32_bf16 v[216:219], a[208:211], v[32:35], v[216:219]\n\t"
    "v_mul_f32 v96, v12, v96\n\t"
    "v_mul_f32 v82, v11, v82\n\t"
    "v_mfma_f32_16x16x32_bf16 v[216:219], a[212:215], v[36:39], v[216:219]\n\t"
    "v_mul_f32 v97, v12, v97\n\t"
    "v_mul_f32 v83, v11, v83\n\t"
    "v_mfma_f32_16x16x32_bf16 v[220:223], a[216:219], v[32:35], v[220:223]\n\t"
    "v_mul_f32 v98, v12, v98\n\t"
    "v_cvt_pk_bf16_f32 v80, v80, v81\n\t"
    "v_mfma_f32_16x16x32_bf16 v[220:223], a[220:223], v[36:39], v[220:223]\n\t"
    "s_waitcnt lgkmcnt(15)\n\t"
    "v_mul_f32 v99, v12, v99\n\t"
    "v_cvt_pk_bf16_f32 v81, v82, v83\n\t"
    "v_mfma_f32_16x16x32_bf16 v[48:51], a[96:99], a[0:3], 0\n\t"
    "ds_write_b64 v7, v[80:81] offset:128\n\t"
    "v_mfma_f32_16x16x32_bf16 v[48:51], a[100:103], a[4:7], v[48:51]\n\t"
    "v_cvt_pk_bf16_f32 v96, v96, v97\n\t"
    "v_cvt_pk_bf16_f32 v97, v98, v99\n\t"
    "v_mfma_f32_16x16x32_bf16 v[48:51], a[104:107], a[8:11], v[48:51]\n\t"
    "ds_write_b64 v7, v[96:97] offset:4352\n\t"
    "v_mfma_f32_16x16x32_bf16 v[48:51], a[108:111], a[12:15], v[48:51]\n\t"
    "v_mul_f32 v84, v11, v84\n\t"
    "v_mul_f32 v85, v11, v85\n\t"
    "v_mfma_f32_16x16x32_bf16 v[52:55], a[112:115], a[0:3], 0\n\t"
    "v_mul_f32 v100, v12, v100\n\t"
    "v_mul_f32 v86, v11, v86\n\t"
    "v_mfma_f32_16x16x32_bf16 v[52:55], a[116:119], a[4:7], v[52:55]\n\t"
    "v_mul_f32 v101, v12, v101\n\t"
    "v_mul_f32 v87, v11, v87\n\t"
    "v_mfma_f32_16x16x32_bf16 v[52:55], a[120:123], a[8:11], v[52:55]\n\t"
    "v_exp_f32 v48, v48\n\t"
    "v_mfma_f32_16x16x32_bf16 v[52:55], a[124:127], a[12:15], v[52:55]\n\t"
    "v_exp_f32 v49, v49\n\t"
    "v_mfma_f32_16x16x32_bf16 v[56:59], a[128:131], a[0:3], 0\n\t"
    "v_exp_f32 v50, v50\n\t"
    "v_mfma_f32_16x16x32_bf16 v[56:59], a[132:135], a[4:7], v[56:59]\n\t"
    "v_exp_f32 v51, v51\n\t"
    "v_mfma_f32_16x16x32_bf16 v[56:59], a[136:139], a[8:11], v[56:59]\n\t"
    "v_cvt_pk_bf16_f32 v32, v48, v49\n\t"
    "v_cvt_pk_bf16_f32 v33, v50, v51\n\t"
    "v_mfma_f32_16x16x32_bf16 v[56:59], a[140:143], a[12:15], v[56:59]\n\t"
    "v_exp_f32 v52, v52\n\t"
    "v_mfma_f32_16x16x32_bf16 v[60:63], a[144:147], a[0:3], 0\n\t"
    "v_exp_f32 v53, v53\n\t"
    "v_mfma_f32_16x16x32_bf16 v[60:63], a[148:151], a[4:7], v[60:63]\n\t"
    "v_exp_f32 v54, v54\n\t"
    "v_mfma_f32_16x16x32_bf16 v[60:63], a[152:155], a[8:11], v[60:63]\n\t"
    "v_exp_f32 v55, v55\n\t"
    "v_mfma_f32_16x16x32_bf16 v[60:63], a[156:159], a[12:15], v[60:63]\n\t"
    "v_cvt_pk_bf16_f32 v34, v52, v53\n\t"
    "v_cvt_pk_bf16_f32 v35, v54, v55\n\t"
    "v_smfmac_f32_16x16x64_bf16 a[244:247], a[248:251], v[40:47], v3\n\t"
    "v_exp_f32 v56, v56\n\t"
    "v_mfma_f32_16x16x32_bf16 v[224:227], a[160:163], v[40:43], v[224:227]\n\t"
    "v_exp_f32 v57, v57\n\t"
    "v_mfma_f32_16x16x32_bf16 v[224:227], a[164:167], v[44:47], v[224:227]\n\t"
    "v_exp_f32 v58, v58\n\t"
    "v_mfma_f32_16x16x32_bf16 v[228:231], a[168:171], v[40:43], v[228:231]\n\t"
    "v_exp_f32 v59, v59\n\t"
    "v_mfma_f32_16x16x32_bf16 v[228:231], a[172:175], v[44:47], v[228:231]\n\t"
    "v_cvt_pk_bf16_f32 v36, v56, v57\n\t"
    "v_cvt_pk_bf16_f32 v37, v58, v59\n\t"
    "v_mfma_f32_16x16x32_bf16 v[232:235], a[176:179], v[40:43], v[232:235]\n\t"
    "v_exp_f32 v60, v60\n\t"
    "v_mfma_f32_16x16x32_bf16 v[232:235], a[180:183], v[44:47], v[232:235]\n\t"
    "v_exp_f32 v61, v61\n\t"
    "v_mfma_f32_16x16x32_bf16 v[236:239], a[184:187], v[40:43], v[236:239]\n\t"
    "v_exp_f32 v62, v62\n\t"
    "v_mfma_f32_16x16x32_bf16 v[236:239], a[188:191], v[44:47], v[236:239]\n\t"
    "v_exp_f32 v63, v63\n\t"
    "v_mfma_f32_16x16x32_bf16 v[240:243], a[192:195], v[40:43], v[240:243]\n\t"
    "v_cvt_pk_bf16_f32 v38, v60, v61\n\t"
    "v_cvt_pk_bf16_f32 v39, v62, v63\n\t"
    "v_mfma_f32_16x16x32_bf16 v[240:243], a[196:199], v[44:47], v[240:243]\n\t"
    "v_mul_f32 v102, v12, v102\n\t"
    "v_cvt_pk_bf16_f32 v84, v84, v85\n\t"
    "v_mfma_f32_16x16x32_bf16 v[244:247], a[200:203], v[40:43], v[244:247]\n\t"
    "v_mul_f32 v103, v12, v103\n\t"
    "v_cvt_pk_bf16_f32 v85, v86, v87\n\t"
    "v_mfma_f32_16x16x32_bf16 v[244:247], a[204:207], v[44:47], v[244:247]\n\t"
    "ds_write_b64 v7, v[84:85] offset:160\n\t"
    "v_mfma_f32_16x16x32_bf16 v[248:251], a[208:211], v[40:43], v[248:251]\n\t"
    "v_cvt_pk_bf16_f32 v100, v100, v101\n\t"
    "v_cvt_pk_bf16_f32 v101, v102, v103\n\t"
    "v_mfma_f32_16x16x32_bf16 v[248:251], a[212:215], v[44:47], v[248:251]\n\t"
    "ds_write_b64 v7, v[100:101] offset:4384\n\t"
    "v_mfma_f32_16x16x32_bf16 v[252:255], a[216:219], v[40:43], v[252:255]\n\t"
    "v_mul_f32 v88, v11, v88\n\t"
    "v_mul_f32 v89, v11, v89\n\t"
    "v_mfma_f32_16x16x32_bf16 v[252:255], a[220:223], v[44:47], v[252:255]\n\t"
    "v_mul_f32 v104, v12, v104\n\t"
    "v_mul_f32 v90, v11, v90\n\t"
    "v_mfma_f32_16x16x32_bf16 v[48:51], a[96:99], a[16:19], 0\n\t"
    "v_mul_f32 v105, v12, v105\n\t"
    "v_mul_f32 v91, v11, v91\n\t"
    "v_mfma_f32_16x16x32_bf16 v[48:51], a[100:103], a[20:23], v[48:51]\n\t"
    "v_mul_f32 v106, v12, v106\n\t"
    "v_cvt_pk_bf16_f32 v88, v88, v89\n\t"
    "v_mfma_f32_16x16x32_bf16 v[48:51], a[104:107], a[24:27], v[48:51]\n\t"
    "v_mul_f32 v107, v12, v107\n\t"
    "v_cvt_pk_bf16_f32 v89, v90, v91\n\t"
    "v_mfma_f32_16x16x32_bf16 v[48:51], a[108:111], a[28:31], v[48:51]\n\t"
    "ds_write_b64 v7, v[88:89] offset:192\n\t"
    "v_mfma_f32_16x16x32_bf16 v[52:55], a[112:115], a[16:19], 0\n\t"
    "v_cvt_pk_bf16_f32 v104, v104, v105\n\t"
    "v_cvt_pk_bf16_f32 v105, v106, v107\n\t"
    "v_mfma_f32_16x16x32_bf16 v[52:55], a[116:119], a[20:23], v[52:55]\n\t"
    "ds_write_b64 v7, v[104:105] offset:4416\n\t"
    "v_mfma_f32_16x16x32_bf16 v[52:55], a[120:123], a[24:27], v[52:55]\n\t"
    "v_mul_f32 v92, v11, v92\n\t"
    "v_mul_f32 v93, v11, v93\n\t"
    "v_mfma_f32_16x16x32_bf16 v[52:55], a[124:127], a[28:31], v[52:55]\n\t"
    "v_exp_f32 v48, v48\n\t"
    "v_mfma_f32_16x16x32_bf16 v[56:59], a[128:131], a[16:19], 0\n\t"
    "v_exp_f32 v49, v49\n\t"
    "v_mfma_f32_16x16x32_bf16 v[56:59], a[132:135], a[20:23], v[56:59]\n\t"
    "v_mul_f32 v108, v12, v108\n\t"
    "v_mul_f32 v94, v11, v94\n\t"
    "v_mfma_f32_16x16x32_bf16 v[56:59], a[136:139], a[24:27], v[56:59]\n\t"
    "v_exp_f32 v50, v50\n\t"
    "v_mfma_f32_16x16x32_bf16 v[56:59], a[140:143], a[28:31], v[56:59]\n\t"
    "v_mul_f32 v109, v12, v109\n\t"
    "v_accvgpr_read_b32 v15, a233\n\t"
    "v_mfma_f32_16x16x32_bf16 v[60:63], a[144:147], a[16:19], 0\n\t"
    "v_exp_f32 v51, v51\n\t"
    "v_mfma_f32_16x16x32_bf16 v[60:63], a[148:151], a[20:23], v[60:63]\n\t"
    "v_mul_f32 v95, v11, v95\n\t"
    "v_mul_f32 v110, v12, v110\n\t"
    "v_mfma_f32_16x16x32_bf16 v[60:63], a[152:155], a[24:27], v[60:63]\n\t"
    "v_add_f32 v13, v14, v15\n\t"
    "v_cvt_pk_bf16_f32 v40, v48, v49\n\t"
    "v_mfma_f32_16x16x32_bf16 v[60:63], a[156:159], a[28:31], v[60:63]\n\t"
    "v_cvt_pk_bf16_f32 v41, v50, v51\n\t"
    "v_cvt_pk_bf16_f32 v92, v92, v93\n\t"
    "v_smfmac_f32_16x16x64_bf16 a[224:227], a[248:251], v[32:39], v3\n\t"
    "v_mul_f32 v111, v12, v111\n\t"
    "v_subrev_u32 v14, s54, v13\n\t"
    "v_mfma_f32_16x16x32_bf16 v[64:67], a[160:163], v[32:35], 0\n\t"
    "v_exp_f32 v52, v52\n\t"
    "v_mfma_f32_16x16x32_bf16 v[64:67], a[164:167], v[36:39], v[64:67]\n\t"
    "v_cvt_pk_bf16_f32 v93, v94, v95\n\t"
    "v_cvt_pk_bf16_f32 v108, v108, v109\n\t"
    "v_mfma_f32_16x16x32_bf16 v[68:71], a[168:171], v[32:35], 0\n\t"
    "ds_write_b64 v7, v[92:93] offset:224\n\t"
    "v_mfma_f32_16x16x32_bf16 v[68:71], a[172:175], v[36:39], v[68:71]\n\t"
    "ds_read_b128 v[16:19], v8 offset:0\n\t"
    "ds_read_b128 v[20:23], v8 offset:1088\n\t"
    "s_waitcnt lgkmcnt(1)\n\t"
    "buffer_store_dwordx4 v[16:19], v9, s[48:51], s62 offen offset:0 sc1\n\t"
    "s_nop 1\n\t"
    "ds_read_b128 v[16:19], v8 offset:2176\n\t"
    "s_waitcnt lgkmcnt(1)\n\t"
    "buffer_store_dwordx4 v[20:23], v9, s[48:51], s62 offen offset:1024 sc1\n\t"
    "s_nop 1\n\t"
    "ds_read_b128 v[20:23], v8 offset:3264\n\t"
    "s_waitcnt lgkmcnt(1)\n\t"
    "buffer_store_dwordx4 v[16:19], v9, s[48:51], s62 offen offset:2048 sc1\n\t"
    "s_waitcnt lgkmcnt(0)\n\t"
    "buffer_store_dwordx4 v[20:23], v9, s[48:51], s62 offen offset:3072 sc1\n\t"
    "v_mfma_f32_16x16x32_bf16 v[72:75], a[176:179], v[32:35], 0\n\t"
    "v_max_u32 v10, v10, v14\n\t"
    "v_cvt_pk_bf16_f32 v109, v110, v111\n\t"
    "v_mfma_f32_16x16x32_bf16 v[72:75], a[180:183], v[36:39], v[72:75]\n\t"
    "v_exp_f32 v53, v53\n\t"
    "v_mfma_f32_16x16x32_bf16 v[76:79], a[184:187], v[32:35], 0\n\t"
    "v_rcp_f32 v13, v13\n\t"
    "v_mfma_f32_16x16x32_bf16 v[76:79], a[188:191], v[36:39], v[76:79]\n\t"
    "v_exp_f32 v54, v54\n\t"
    "v_mfma_f32_16x16x32_bf16 v[80:83], a[192:195], v[32:35], 0\n\t"
    "ds_write_b64 v7, v[108:109] offset:4448\n\t"
    "v_mfma_f32_16x16x32_bf16 v[80:83], a[196:199], v[36:39], v[80:83]\n\t"
    "v_exp_f32 v55, v55\n\t"
    "v_mfma_f32_16x16x32_bf16 v[84:87], a[200:203], v[32:35], 0\n\t"
    "v_accvgpr_write_b32 a232, 0\n\t"
    "v_cvt_pk_bf16_f32 v42, v52, v53\n\t"
    "v_mfma_f32_16x16x32_bf16 v[84:87], a[204:207], v[36:39], v[84:87]\n\t"
    "v_cvt_pk_bf16_f32 v43, v54, v55\n\t"
    "v_mul_f32 v112, v12, v112\n\t"
    "v_mfma_f32_16x16x32_bf16 v[88:91], a[208:211], v[32:35], 0\n\t"
    "v_accvgpr_write_b32 a233, 0\n\t"
    "v_mul_f32 v113, v12, v113\n\t"
    "v_mfma_f32_16x16x32_bf16 v[88:91], a[212:215], v[36:39], v[88:91]\n\t"
    "v_exp_f32 v56, v56\n\t"
    "v_mfma_f32_16x16x32_bf16 v[92:95], a[216:219], v[32:35], 0\n\t"
    "v_mul_f32 v128, v13, v128\n\t"
    "v_mul_f32 v114, v12, v114\n\t"
    "v_mfma_f32_16x16x32_bf16 v[92:95], a[220:223], v[36:39], v[92:95]\n\t"
    "v_exp_f32 v57, v57\n\t"
    "v_mfma_f32_16x16x32_bf16 v[48:51], a[96:99], a[32:35], 0\n\t"
    "v_mul_f32 v129, v13, v129\n\t"
    "v_mul_f32 v115, v12, v115\n\t"
    "v_mfma_f32_16x16x32_bf16 v[48:51], a[100:103], a[36:39], v[48:51]\n\t"
    "v_mul_f32 v130, v13, v130\n\t"
    "v_cvt_pk_bf16_f32 v112, v112, v113\n\t"
    "v_mfma_f32_16x16x32_bf16 v[48:51], a[104:107], a[40:43], v[48:51]\n\t"
    "v_mul_f32 v131, v13, v131\n\t"
    "v_cvt_pk_bf16_f32 v113, v114, v115\n\t"
    "v_mfma_f32_16x16x32_bf16 v[48:51], a[108:111], a[44:47], v[48:51]\n\t"
    "ds_write_b64 v7, v[112:113] offset:4480\n\t"
    "v_mfma_f32_16x16x32_bf16 v[52:55], a[112:115], a[32:35], 0\n\t"
    "v_exp_f32 v58, v58\n\t"
    "v_mfma_f32_16x16x32_bf16 v[52:55], a[116:119], a[36:39], v[52:55]\n\t"
    "v_exp_f32 v59, v59\n\t"
    "v_mfma_f32_16x16x32_bf16 v[52:55], a[120:123], a[40:43], v[52:55]\n\t"
    "v_cvt_pk_bf16_f32 v44, v56, v57\n\t"
    "v_cvt_pk_bf16_f32 v45, v58, v59\n\t"
    "v_mfma_f32_16x16x32_bf16 v[52:55], a[124:127], a[44:47], v[52:55]\n\t"
    "v_cvt_pk_bf16_f32 v128, v128, v129\n\t"
    "v_cvt_pk_bf16_f32 v129, v130, v131\n\t"
    "v_mfma_f32_16x16x32_bf16 v[56:59], a[128:131], a[32:35], 0\n\t"
    "v_exp_f32 v60, v60\n\t"
    "v_mfma_f32_16x16x32_bf16 v[56:59], a[132:135], a[36:39], v[56:59]\n\t"
    "v_exp_f32 v61, v61\n\t"
    "v_mfma_f32_16x16x32_bf16 v[56:59], a[136:139], a[40:43], v[56:59]\n\t"
    "ds_write_b64 v7, v[128:129] offset:8704\n\t"
    "v_exp_f32 v62, v62\n\t"
    "v_exp_f32 v63, v63\n\t"
    "v_mfma_f32_16x16x32_bf16 v[56:59], a[140:143], a[44:47], v[56:59]\n\t"
    "v_mul_f32 v116, v12, v116\n\t"
    "v_mul_f32 v117, v12, v117\n\t"
    "v_cvt_pk_bf16_f32 v46, v60, v61\n\t"
    "v_cvt_pk_bf16_f32 v47, v62, v63\n\t"
    "v_mfma_f32_16x16x32_bf16 v[60:63], a[144:147], a[32:35], 0\n\t"
    "v_mul_f32 v132, v13, v132\n\t"
    "v_mul_f32 v118, v12, v118\n\t"
    "v_mfma_f32_16x16x32_bf16 v[60:63], a[148:151], a[36:39], v[60:63]\n\t"
    "v_mul_f32 v133, v13, v133\n\t"
    "v_mul_f32 v119, v12, v119\n\t"
    "v_mfma_f32_16x16x32_bf16 v[60:63], a[152:155], a[40:43], v[60:63]\n\t"
    "v_mul_f32 v134, v13, v134\n\t"
    "v_cvt_pk_bf16_f32 v116, v116, v117\n\t"
    "v_mfma_f32_16x16x32_bf16 v[60:63], a[156:159], a[44:47], v[60:63]\n\t"
    "v_mul_f32 v135, v13, v135\n\t"
    "v_cvt_pk_bf16_f32 v117, v118, v119\n\t"
    "v_smfmac_f32_16x16x64_bf16 a[228:231], a[248:251], v[40:47], v3\n\t"
    "ds_write_b64 v7, v[116:117] offset:4512\n\t"
    "v_mfma_f32_16x16x32_bf16 v[96:99], a[160:163], v[40:43], 0\n\t"
    "v_cvt_pk_bf16_f32 v132, v132, v133\n\t"
    "v_cvt_pk_bf16_f32 v133, v134, v135\n\t"
    "v_mfma_f32_16x16x32_bf16 v[96:99], a[164:167], v[44:47], v[96:99]\n\t"
    "ds_write_b64 v7, v[132:133] offset:8736\n\t"
    "v_mfma_f32_16x16x32_bf16 v[100:103], a[168:171], v[40:43], 0\n\t"
    "v_mul_f32 v120, v12, v120\n\t"
    "v_mul_f32 v121, v12, v121\n\t"
    "v_mfma_f32_16x16x32_bf16 v[100:103], a[172:175], v[44:47], v[100:103]\n\t"
    "v_mul_f32 v136, v13, v136\n\t"
    "v_mul_f32 v122, v12, v122\n\t"
    "v_mfma_f32_16x16x32_bf16 v[104:107], a[176:179], v[40:43], 0\n\t"
    "v_mul_f32 v137, v13, v137\n\t"
    "v_mul_f32 v123, v12, v123\n\t"
    "v_mfma_f32_16x16x32_bf16 v[104:107], a[180:183], v[44:47], v[104:107]\n\t"
    "v_mul_f32 v138, v13, v138\n\t"
    "v_cvt_pk_bf16_f32 v120, v120, v121\n\t"
    "v_mfma_f32_16x16x32_bf16 v[108:111], a[184:187], v[40:43], 0\n\t"
    "v_mul_f32 v139, v13, v139\n\t"
    "v_cvt_pk_bf16_f32 v121, v122, v123\n\t"
    "v_mfma_f32_16x16x32_bf16 v[108:111], a[188:191], v[44:47], v[108:111]\n\t"
    "ds_write_b64 v7, v[120:121] offset:4544\n\t"
    "v_mfma_f32_16x16x32_bf16 v[112:115], a[192:195], v[40:43], 0\n\t"
    "v_cvt_pk_bf16_f32 v136, v136, v137\n\t"
    "v_cvt_pk_bf16_f32 v137, v138, v139\n\t"
    "v_mfma_f32_16x16x32_bf16 v[112:115], a[196:199], v[44:47], v[112:115]\n\t"
    "ds_write_b64 v7, v[136:137] offset:8768\n\t"
    "v_mfma_f32_16x16x32_bf16 v[116:119], a[200:203], v[40:43], 0\n\t"
    "v_exp_f32 v48, v48\n\t"
    "v_mfma_f32_16x16x32_bf16 v[116:119], a[204:207], v[44:47], v[116:119]\n\t"
    "v_mul_f32 v124, v12, v124\n\t"
    "v_mul_f32 v125, v12, v125\n\t"
    "v_mul_f32 v126, v12, v126\n\t"
    "v_mul_f32 v127, v12, v127\n\t"
    "v_cvt_pk_bf16_f32 v124, v124, v125\n\t"
    "v_cvt_pk_bf16_f32 v125, v126, v127\n\t"
    "ds_write_b64 v7, v[124:125] offset:4576\n\t"
    "v_mfma_f32_16x16x32_bf16 v[120:123], a[208:211], v[40:43], 0\n\t"
    "v_exp_f32 v49, v49\n\t"
    "v_mfma_f32_16x16x32_bf16 v[120:123], a[212:215], v[44:47], v[120:123]\n\t"
    "v_mul_f32 v140, v13, v140\n\t"
    "v_accvgpr_read_b32 v14, a236\n\t"
    "v_mfma_f32_16x16x32_bf16 v[124:127], a[216:219], v[40:43], 0\n\t"
    "v_exp_f32 v50, v50\n\t"
    "v_exp_f32 v51, v51\n\t"
    "v_mfma_f32_16x16x32_bf16 v[124:127], a[220:223], v[44:47], v[124:127]\n\t"
    "v_mul_f32 v141, v13, v141\n\t"
    "v_accvgpr_read_b32 v15, a237\n\t"
    "v_cvt_pk_bf16_f32 v32, v48, v49\n\t"
    "v_cvt_pk_bf16_f32 v33, v50, v51\n\t"
    "v_mfma_f32_16x16x32_bf16 v[48:51], a[96:99], a[48:51], 0\n\t"
    "v_mul_f32 v142, v13, v142\n\t"
    "v_add_f32 v11, v14, v15\n\t"
    "v_mfma_f32_16x16x32_bf16 v[48:51], a[100:103], a[52:55], v[48:51]\n\t"
    "v_mul_f32 v143, v13, v143\n\t"
    "v_subrev_u32 v14, s54, v11\n\t"
    "v_mfma_f32_16x16x32_bf16 v[48:51], a[104:107], a[56:59], v[48:51]\n\t"
    "v_exp_f32 v52, v52\n\t"
    "v_exp_f32 v53, v53\n\t"
    "v_exp_f32 v54, v54\n\t"
    "v_exp_f32 v55, v55\n\t"
    "v_mfma_f32_16x16x32_bf16 v[48:51], a[108:111], a[60:63], v[48:51]\n\t"
    "v_cvt_pk_bf16_f32 v140, v140, v141\n\t"
    "v_max_u32 v10, v10, v14\n\t"
    "v_cvt_pk_bf16_f32 v34, v52, v53\n\t"
    "v_cvt_pk_bf16_f32 v35, v54, v55\n\t"
    "v_mfma_f32_16x16x32_bf16 v[52:55], a[112:115], a[48:51], 0\n\t"
    "v_cvt_pk_bf16_f32 v141, v142, v143\n\t"
    "v_accvgpr_read_b32 v14, a240\n\t"
    "v_mfma_f32_16x16x32_bf16 v[52:55], a[116:119], a[52:55], v[52:55]\n\t"
    "v_rcp_f32 v11, v11\n\t"
    "v_mfma_f32_16x16x32_bf16 v[52:55], a[120:123], a[56:59], v[52:55]\n\t"
    "ds_write_b64 v7, v[140:141] offset:8800\n\t"
    "v_exp_f32 v56, v56\n\t"
    "v_exp_f32 v57, v57\n\t"
    "v_exp_f32 v58, v58\n\t"
    "v_exp_f32 v59, v59\n\t"
    "v_mfma_f32_16x16x32_bf16 v[52:55], a[124:127], a[60:63], v[52:55]\n\t"
    "v_accvgpr_write_b32 a236, 0\n\t"
    "v_mul_f32 v144, v13, v144\n\t"
    "v_cvt_pk_bf16_f32 v36, v56, v57\n\t"
    "v_cvt_pk_bf16_f32 v37, v58, v59\n\t"
    "v_mfma_f32_16x16x32_bf16 v[56:59], a[128:131], a[48:51], 0\n\t"
    "v_accvgpr_write_b32 a237, 0\n\t"
    "v_mul_f32 v145, v13, v145\n\t"
    "v_mfma_f32_16x16x32_bf16 v[56:59], a[132:135], a[52:55], v[56:59]\n\t"
    "v_mul_f32 v160, v11, v160\n\t"
    "v_mul_f32 v146, v13, v146\n\t"
    "v_mfma_f32_16x16x32_bf16 v[56:59], a[136:139], a[56:59], v[56:59]\n\t"
    "v_mul_f32 v161, v11, v161\n\t"
    "v_mul_f32 v147, v13, v147\n\t"
    "v_exp_f32 v60, v60\n\t"
    "v_exp_f32 v61, v61\n\t"
    "v_exp_f32 v62, v62\n\t"
    "v_exp_f32 v63, v63\n\t"
    "v_mfma_f32_16x16x32_bf16 v[56:59], a[140:143], a[60:63], v[56:59]\n\t"
    "v_mul_f32 v162, v11, v162\n\t"
    "v_cvt_pk_bf16_f32 v144, v144, v145\n\t"
    "v_cvt_pk_bf16_f32 v38, v60, v61\n\t"
    "v_cvt_pk_bf16_f32 v39, v62, v63\n\t"
    "v_mfma_f32_16x16x32_bf16 v[60:63], a[144:147], a[48:51], 0\n\t"
    "v_mul_f32 v163, v11, v163\n\t"
    "v_cvt_pk_bf16_f32 v145, v146, v147\n\t"
    "v_mfma_f32_16x16x32_bf16 v[60:63], a[148:151], a[52:55], v[60:63]\n\t"
    "ds_write_b64 v7, v[144:145] offset:8832\n\t"
    "v_mfma_f32_16x16x32_bf16 v[60:63], a[152:155], a[56:59], v[60:63]\n\t"
    "v_cvt_pk_bf16_f32 v160, v160, v161\n\t"
    "v_cvt_pk_bf16_f32 v161, v162, v163\n\t"
    "v_mfma_f32_16x16x32_bf16 v[60:63], a[156:159], a[60:63], v[60:63]\n\t"
    "ds_write_b64 v7, v[160:161] offset:13056\n\t"
    "v_smfmac_f32_16x16x64_bf16 a[232:235], a[248:251], v[32:39], v3\n\t"
    "v_mul_f32 v148, v13, v148\n\t"
    "v_mul_f32 v149, v13, v149\n\t"
    "v_mfma_f32_16x16x32_bf16 v[128:131], a[160:163], v[32:35], 0\n\t"
    "v_mul_f32 v164, v11, v164\n\t"
    "v_mul_f32 v150, v13, v150\n\t"
    "v_mfma_f32_16x16x32_bf16 v[128:131], a[164:167], v[36:39], v[128:131]\n\t"
    "v_mul_f32 v165, v11, v165\n\t"
    "v_mul_f32 v151, v13, v151\n\t"
    "v_mfma_f32_16x16x32_bf16 v[132:135], a[168:171], v[32:35], 0\n\t"
    "v_mul_f32 v166, v11, v166\n\t"
    "v_cvt_pk_bf16_f32 v148, v148, v149\n\t"
    "v_mfma_f32_16x16x32_bf16 v[132:135], a[172:175], v[36:39], v[132:135]\n\t"
    "v_mul_f32 v167, v11, v167\n\t"
    "v_cvt_pk_bf16_f32 v149, v150, v151\n\t"
    "v_mfma_f32_16x16x32_bf16 v[136:139], a[176:179], v[32:35], 0\n\t"
    "ds_write_b64 v7, v[148:149] offset:8864\n\t"
    "v_mfma_f32_16x16x32_bf16 v[136:139], a[180:183], v[36:39], v[136:139]\n\t"
    "v_cvt_pk_bf16_f32 v164, v164, v165\n\t"
    "v_cvt_pk_bf16_f32 v165, v166, v167\n\t"
    "v_mfma_f32_16x16x32_bf16 v[140:143], a[184:187], v[32:35], 0\n\t"
    "ds_write_b64 v7, v[164:165] offset:13088\n\t"
    "v_mfma_f32_16x16x32_bf16 v[140:143], a[188:191], v[36:39], v[140:143]\n\t"
    "v_mul_f32 v152, v13, v152\n\t"
    "v_mul_f32 v153, v13, v153\n\t"
    "v_mfma_f32_16x16x32_bf16 v[144:147], a[192:195], v[32:35], 0\n\t"
    "v_mul_f32 v168, v11, v168\n\t"
    "v_mul_f32 v154, v13, v154\n\t"
    "v_mfma_f32_16x16x32_bf16 v[144:147], a[196:199], v[36:39], v[144:147]\n\t"
    "v_mul_f32 v169, v11, v169\n\t"
    "v_mul_f32 v155, v13, v155\n\t"
    "v_cvt_pk_bf16_f32 v152, v152, v153\n\t"
    "v_cvt_pk_bf16_f32 v153, v154, v155\n\t"
    "ds_write_b64 v7, v[152:153] offset:8896\n\t"
    "v_mfma_f32_16x16x32_bf16 v[148:151], a[200:203], v[32:35], 0\n\t"
    "v_mul_f32 v170, v11, v170\n\t"
    "v_mul_f32 v171, v11, v171\n\t"
    "v_mfma_f32_16x16x32_bf16 v[148:151], a[204:207], v[36:39], v[148:151]\n\t"
    "v_cvt_pk_bf16_f32 v168, v168, v169\n\t"
    "v_cvt_pk_bf16_f32 v169, v170, v171\n\t"
    "v_mul_f32 v156, v13, v156\n\t"
    "v_mul_f32 v157, v13, v157\n\t"
    "v_mul_f32 v158, v13, v158\n\t"
    "v_mul_f32 v159, v13, v159\n\t"
    "v_cvt_pk_bf16_f32 v156, v156, v157\n\t"
    "v_cvt_pk_bf16_f32 v157, v158, v159\n\t"
    "ds_write_b64 v7, v[156:157] offset:8928\n\t"
    "v_mfma_f32_16x16x32_bf16 v[152:155], a[208:211], v[32:35], 0\n\t"
    "ds_write_b64 v7, v[168:169] offset:13120\n\t"
    "v_mfma_f32_16x16x32_bf16 v[152:155], a[212:215], v[36:39], v[152:155]\n\t"
    "v_exp_f32 v48, v48\n\t"
    "v_mfma_f32_16x16x32_bf16 v[156:159], a[216:219], v[32:35], 0\n\t"
    "v_exp_f32 v49, v49\n\t"
    "v_exp_f32 v50, v50\n\t"
    "v_exp_f32 v51, v51\n\t"
    "v_mfma_f32_16x16x32_bf16 v[156:159], a[220:223], v[36:39], v[156:159]\n\t"
    "v_mul_f32 v172, v11, v172\n\t"
    "v_mul_f32 v173, v11, v173\n\t"
    "v_cvt_pk_bf16_f32 v40, v48, v49\n\t"
    "v_cvt_pk_bf16_f32 v41, v50, v51\n\t"
    "v_mfma_f32_16x16x32_bf16 v[48:51], a[96:99], a[64:67], 0\n\t"
    "v_accvgpr_read_b32 v15, a241\n\t"
    "v_mul_f32 v174, v11, v174\n\t"
    "v_mfma_f32_16x16x32_bf16 v[48:51], a[100:103], a[68:71], v[48:51]\n\t"
    "v_add_f32 v12, v14, v15\n\t"
    "v_mul_f32 v175, v11, v175\n\t"
    "v_mfma_f32_16x16x32_bf16 v[48:51], a[104:107], a[72:75], v[48:51]\n\t"
    "v_subrev_u32 v14, s54, v12\n\t"
    "v_exp_f32 v52, v52\n\t"
    "v_exp_f32 v53, v53\n\t"
    "v_exp_f32 v54, v54\n\t"
    "v_exp_f32 v55, v55\n\t"
    "v_mfma_f32_16x16x32_bf16 v[48:51], a[108:111], a[76:79], v[48:51]\n\t"
    "v_cvt_pk_bf16_f32 v172, v172, v173\n\t"
    "v_max_u32 v10, v10, v14\n\t"
    "v_cvt_pk_bf16_f32 v42, v52, v53\n\t"
    "v_cvt_pk_bf16_f32 v43, v54, v55\n\t"
    "v_mfma_f32_16x16x32_bf16 v[52:55], a[112:115], a[64:67], 0\n\t"
    "v_cvt_pk_bf16_f32 v173, v174, v175\n\t"
    "v_accvgpr_read_b32 v14, a244\n\t"
    "v_mfma_f32_16x16x32_bf16 v[52:55], a[116:119], a[68:71], v[52:55]\n\t"
    "v_rcp_f32 v12, v12\n\t"
    "v_mfma_f32_16x16x32_bf16 v[52:55], a[120:123], a[72:75], v[52:55]\n\t"
    "ds_write_b64 v7, v[172:173] offset:13152\n\t"
    "v_exp_f32 v56, v56\n\t"
    "v_exp_f32 v57, v57\n\t"
    "v_exp_f32 v58, v58\n\t"
    "v_exp_f32 v59, v59\n\t"
    "v_mfma_f32_16x16x32_bf16 v[52:55], a[124:127], a[76:79], v[52:55]\n\t"
    "v_accvgpr_write_b32 a240, 0\n\t"
    "v_mul_f32 v176, v11, v176\n\t"
    "v_cvt_pk_bf16_f32 v44, v56, v57\n\t"
    "v_cvt_pk_bf16_f32 v45, v58, v59\n\t"
    "v_mfma_f32_16x16x32_bf16 v[56:59], a[128:131], a[64:67], 0\n\t"
    "v_accvgpr_write_b32 a241, 0\n\t"
    "v_mul_f32 v177, v11, v177\n\t"
    "v_mfma_f32_16x16x32_bf16 v[56:59], a[132:135], a[68:71], v[56:59]\n\t"
    "v_mul_f32 v192, v12, v192\n\t"
    "v_mul_f32 v178, v11, v178\n\t"
    "v_mfma_f32_16x16x32_bf16 v[56:59], a[136:139], a[72:75], v[56:59]\n\t"
    "v_mul_f32 v193, v12, v193\n\t"
    "v_mul_f32 v179, v11, v179\n\t"
    "v_exp_f32 v60, v60\n\t"
    "v_exp_f32 v61, v61\n\t"
    "v_exp_f32 v62, v62\n\t"
    "v_exp_f32 v63, v63\n\t"
    "v_mfma_f32_16x16x32_bf16 v[56:59], a[140:143], a[76:79], v[56:59]\n\t"
    "v_mul_f32 v194, v12, v194\n\t"
    "v_cvt_pk_bf16_f32 v176, v176, v177\n\t"
    "v_cvt_pk_bf16_f32 v46, v60, v61\n\t"
    "v_cvt_pk_bf16_f32 v47, v62, v63\n\t"
    "v_mfma_f32_16x16x32_bf16 v[60:63], a[144:147], a[64:67], 0\n\t"
    "v_mul_f32 v195, v12, v195\n\t"
    "v_cvt_pk_bf16_f32 v177, v178, v179\n\t"
    "v_mfma_f32_16x16x32_bf16 v[60:63], a[148:151], a[68:71], v[60:63]\n\t"
    "ds_write_b64 v7, v[176:177] offset:13184\n\t"
    "v_mfma_f32_16x16x32_bf16 v[60:63], a[152:155], a[72:75], v[60:63]\n\t"
    "v_cvt_pk_bf16_f32 v192, v192, v193\n\t"
    "v_cvt_pk_bf16_f32 v193, v194, v195\n\t"
    "v_mfma_f32_16x16x32_bf16 v[60:63], a[156:159], a[76:79], v[60:63]\n\t"
    "ds_write_b64 v7, v[192:193] offset:17408\n\t"
    "v_smfmac_f32_16x16x64_bf16 a[236:239], a[248:251], v[40:47], v3\n\t"
    "v_mul_f32 v180, v11, v180\n\t"
    "v_mul_f32 v181, v11, v181\n\t"
    "v_mfma_f32_16x16x32_bf16 v[160:163], a[160:163], v[40:43], 0\n\t"
    "v_mul_f32 v196, v12, v196\n\t"
    "v_mul_f32 v182, v11, v182\n\t"
    "v_mfma_f32_16x16x32_bf16 v[160:163], a[164:167], v[44:47], v[160:163]\n\t"
    "v_mul_f32 v197, v12, v197\n\t"
    "v_mul_f32 v183, v11, v183\n\t"
    "v_mfma_f32_16x16x32_bf16 v[164:167], a[168:171], v[40:43], 0\n\t"
    "v_mul_f32 v198, v12, v198\n\t"
    "v_cvt_pk_bf16_f32 v180, v180, v181\n\t"
    "v_mfma_f32_16x16x32_bf16 v[164:167], a[172:175], v[44:47], v[164:167]\n\t"
    "v_mul_f32 v199, v12, v199\n\t"
    "v_cvt_pk_bf16_f32 v181, v182, v183\n\t"
    "v_mfma_f32_16x16x32_bf16 v[168:171], a[176:179], v[40:43], 0\n\t"
    "ds_write_b64 v7, v[180:181] offset:13216\n\t"
    "v_mfma_f32_16x16x32_bf16 v[168:171], a[180:183], v[44:47], v[168:171]\n\t"
    "v_cvt_pk_bf16_f32 v196, v196, v197\n\t"
    "v_cvt_pk_bf16_f32 v197, v198, v199\n\t"
    "v_mfma_f32_16x16x32_bf16 v[172:175], a[184:187], v[40:43], 0\n\t"
    "ds_write_b64 v7, v[196:197] offset:17440\n\t"
    "v_mfma_f32_16x16x32_bf16 v[172:175], a[188:191], v[44:47], v[172:175]\n\t"
    "v_mul_f32 v184, v11, v184\n\t"
    "v_mul_f32 v185, v11, v185\n\t"
    "v_mfma_f32_16x16x32_bf16 v[176:179], a[192:195], v[40:43], 0\n\t"
    "v_mul_f32 v200, v12, v200\n\t"
    "v_mul_f32 v186, v11, v186\n\t"
    "v_mfma_f32_16x16x32_bf16 v[176:179], a[196:199], v[44:47], v[176:179]\n\t"
    "v_mul_f32 v201, v12, v201\n\t"
    "v_mul_f32 v187, v11, v187\n\t"
    "v_cvt_pk_bf16_f32 v184, v184, v185\n\t"
    "v_cvt_pk_bf16_f32 v185, v186, v187\n\t"
    "ds_write_b64 v7, v[184:185] offset:13248\n\t"
    "v_mfma_f32_16x16x32_bf16 v[180:183], a[200:203], v[40:43], 0\n\t"
    "v_mul_f32 v202, v12, v202\n\t"
    "v_mul_f32 v203, v12, v203\n\t"
    "v_mfma_f32_16x16x32_bf16 v[180:183], a[204:207], v[44:47], v[180:183]\n\t"
    "v_cvt_pk_bf16_f32 v200, v200, v201\n\t"
    "v_cvt_pk_bf16_f32 v201, v202, v203\n\t"
    "v_mul_f32 v188, v11, v188\n\t"
    "v_mul_f32 v189, v11, v189\n\t"
    "v_mul_f32 v190, v11, v190\n\t"
    "v_mul_f32 v191, v11, v191\n\t"
    "v_cvt_pk_bf16_f32 v188, v188, v189\n\t"
    "v_cvt_pk_bf16_f32 v189, v190, v191\n\t"
    "ds_write_b64 v7, v[188:189] offset:13280\n\t"
    "v_mfma_f32_16x16x32_bf16 v[184:187], a[208:211], v[40:43], 0\n\t"
    "ds_write_b64 v7, v[200:201] offset:17472\n\t"
    "v_mfma_f32_16x16x32_bf16 v[184:187], a[212:215], v[44:47], v[184:187]\n\t"
    "v_exp_f32 v48, v48\n\t"
    "v_mfma_f32_16x16x32_bf16 v[188:191], a[216:219], v[40:43], 0\n\t"
    "v_exp_f32 v49, v49\n\t"
    "v_exp_f32 v50, v50\n\t"
    "v_exp_f32 v51, v51\n\t"
    "v_mfma_f32_16x16x32_bf16 v[188:191], a[220:223], v[44:47], v[188:191]\n\t"
    "v_mul_f32 v204, v12, v204\n\t"
    "v_mul_f32 v205, v12, v205\n\t"
    "v_cvt_pk_bf16_f32 v32, v48, v49\n\t"
    "v_cvt_pk_bf16_f32 v33, v50, v51\n\t"
    "v_mfma_f32_16x16x32_bf16 v[48:51], a[96:99], a[80:83], 0\n\t"
    "ds_read_b128 a[96:99], v0 offset:0\n\t"
    "v_accvgpr_read_b32 v15, a245\n\t"
    "v_mfma_f32_16x16x32_bf16 v[48:51], a[100:103], a[84:87], v[48:51]\n\t"
    "ds_read_b128 a[100:103], v0 offset:1024\n\t"
    "v_mul_f32 v206, v12, v206\n\t"
    "v_mfma_f32_16x16x32_bf16 v[48:51], a[104:107], a[88:91], v[48:51]\n\t"
    "ds_read_b128 a[104:107], v0 offset:2048\n\t"
    "v_add_f32 v13, v14, v15\n\t"
    "v_exp_f32 v52, v52\n\t"
    "v_exp_f32 v53, v53\n\t"
    "v_exp_f32 v54, v54\n\t"
    "v_exp_f32 v55, v55\n\t"
    "v_mfma_f32_16x16x32_bf16 v[48:51], a[108:111], a[92:95], v[48:51]\n\t"
    "ds_read_b128 a[108:111], v0 offset:3072\n\t"
    "v_mul_f32 v207, v12, v207\n\t"
    "v_cvt_pk_bf16_f32 v34, v52, v53\n\t"
    "v_cvt_pk_bf16_f32 v35, v54, v55\n\t"
    "v_mfma_f32_16x16x32_bf16 v[52:55], a[112:115], a[80:83], 0\n\t"
    "ds_read_b128 a[112:115], v0 offset:4096\n\t"
    "v_subrev_u32 v14, s54, v13\n\t"
    "v_mfma_f32_16x16x32_bf16 v[52:55], a[116:119], a[84:87], v[52:55]\n\t"
    "ds_read_b128 a[116:119], v0 offset:5120\n\t"
    "v_cvt_pk_bf16_f32 v204, v204, v205\n\t"
    "v_mfma_f32_16x16x32_bf16 v[52:55], a[120:123], a[88:91], v[52:55]\n\t"
    "ds_read_b128 a[120:123], v0 offset:6144\n\t"
    "v_max_u32 v10, v10, v14\n\t"
    "v_exp_f32 v56, v56\n\t"
    "v_exp_f32 v57, v57\n\t"
    "v_exp_f32 v58, v58\n\t"
    "v_exp_f32 v59, v59\n\t"
    "v_mfma_f32_16x16x32_bf16 v[52:55], a[124:127], a[92:95], v[52:55]\n\t"
    "ds_read_b128 a[124:127], v0 offset:7168\n\t"
    "v_cvt_pk_bf16_f32 v205, v206, v207\n\t"
    "v_cvt_pk_bf16_f32 v36, v56, v57\n\t"
    "v_cvt_pk_bf16_f32 v37, v58, v59\n\t"
    "v_mfma_f32_16x16x32_bf16 v[56:59], a[128:131], a[80:83], 0\n\t"
    "v_rcp_f32 v13, v13\n\t"
    "v_mfma_f32_16x16x32_bf16 v[56:59], a[132:135], a[84:87], v[56:59]\n\t"
    "ds_write_b64 v7, v[204:205] offset:17504\n\t"
    "v_mfma_f32_16x16x32_bf16 v[56:59], a[136:139], a[88:91], v[56:59]\n\t"
    "ds_read_b128 a[128:131], v0 offset:8192\n\t"
    "v_accvgpr_write_b32 a244, 0\n\t"
    "v_exp_f32 v60, v60\n\t"
    "v_exp_f32 v61, v61\n\t"
    "v_exp_f32 v62, v62\n\t"
    "v_exp_f32 v63, v63\n\t"
    "v_mfma_f32_16x16x32_bf16 v[56:59], a[140:143], a[92:95], v[56:59]\n\t"
    "ds_read_b128 a[132:135], v0 offset:9216\n\t"
    "v_mul_f32 v208, v12, v208\n\t"
    "v_cvt_pk_bf16_f32 v38, v60, v61\n\t"
    "v_cvt_pk_bf16_f32 v39, v62, v63\n\t"
    "v_mfma_f32_16x16x32_bf16 v[60:63], a[144:147], a[80:83], 0\n\t"
    "ds_read_b128 a[136:139], v0 offset:10240\n\t"
    "v_accvgpr_write_b32 a245, 0\n\t"
    "v_mfma_f32_16x16x32_bf16 v[60:63], a[148:151], a[84:87], v[60:63]\n\t"
    "ds_read_b128 a[140:143], v0 offset:11264\n\t"
    "v_mul_f32 v209, v12, v209\n\t"
    "v_mfma_f32_16x16x32_bf16 v[60:63], a[152:155], a[88:91], v[60:63]\n\t"
    "ds_read_b128 a[144:147], v0 offset:12288\n\t"
    "v_mul_f32 v224, v13, v224\n\t"
    "v_mfma_f32_16x16x32_bf16 v[60:63], a[156:159], a[92:95], v[60:63]\n\t"
    "ds_read_b128 a[148:151], v0 offset:13312\n\t"
    "v_mul_f32 v210, v12, v210\n\t"
    "v_smfmac_f32_16x16x64_bf16 a[240:243], a[248:251], v[32:39], v3\n\t"
    "ds_read_b128 a[152:155], v0 offset:14336\n\t"
    "v_mul_f32 v225, v13, v225\n\t"
    "v_mfma_f32_16x16x32_bf16 v[192:195], a[160:163], v[32:35], 0\n\t"
    "ds_read_b128 a[156:159], v0 offset:15360\n\t"
    "v_mul_f32 v211, v12, v211\n\t"
    "v_mfma_f32_16x16x32_bf16 v[192:195], a[164:167], v[36:39], v[192:195]\n\t"
    "v_mul_f32 v226, v13, v226\n\t"
    "v_cvt_pk_bf16_f32 v208, v208, v209\n\t"
    "v_mfma_f32_16x16x32_bf16 v[196:199], a[168:171], v[32:35], 0\n\t"
    "v_mul_f32 v227, v13, v227\n\t"
    "v_cvt_pk_bf16_f32 v209, v210, v211\n\t"
    "v_mfma_f32_16x16x32_bf16 v[196:199], a[172:175], v[36:39], v[196:199]\n\t"
    "ds_write_b64 v7, v[208:209] offset:17536\n\t"
    "v_mfma_f32_16x16x32_bf16 v[200:203], a[176:179], v[32:35], 0\n\t"
    "v_cvt_pk_bf16_f32 v224, v224, v225\n\t"
    "v_cvt_pk_bf16_f32 v225, v226, v227\n\t"
    "v_mfma_f32_16x16x32_bf16 v[200:203], a[180:183], v[36:39], v[200:203]\n\t"
    "ds_write_b64 v7, v[224:225] offset:0\n\t"
    "v_mfma_f32_16x16x32_bf16 v[204:207], a[184:187], v[32:35], 0\n\t"
    "v_mul_f32 v212, v12, v212\n\t"
    "v_mul_f32 v213, v12, v213\n\t"
    "v_mfma_f32_16x16x32_bf16 v[204:207], a[188:191], v[36:39], v[204:207]\n\t"
    "v_mul_f32 v228, v13, v228\n\t"
    "v_mul_f32 v214, v12, v214\n\t"
    "v_mul_f32 v215, v12, v215\n\t"
    "v_cvt_pk_bf16_f32 v212, v212, v213\n\t"
    "v_cvt_pk_bf16_f32 v213, v214, v215\n\t"
    "ds_write_b64 v7, v[212:213] offset:17568\n\t"
    "v_mfma_f32_16x16x32_bf16 v[208:211], a[192:195], v[32:35], 0\n\t"
    "v_mul_f32 v229, v13, v229\n\t"
    "v_mul_f32 v230, v13, v230\n\t"
    "v_mfma_f32_16x16x32_bf16 v[208:211], a[196:199], v[36:39], v[208:211]\n\t"
    "v_mul_f32 v231, v13, v231\n\t"
    "v_cvt_pk_bf16_f32 v228, v228, v229\n\t"
    "v_mul_f32 v216, v12, v216\n\t"
    "v_mul_f32 v217, v12, v217\n\t"
    "v_mul_f32 v218, v12, v218\n\t"
    "v_mul_f32 v219, v12, v219\n\t"
    "v_cvt_pk_bf16_f32 v216, v216, v217\n\t"
    "v_cvt_pk_bf16_f32 v217, v218, v219\n\t"
    "ds_write_b64 v7, v[216:217] offset:17600\n\t"
    "v_mfma_f32_16x16x32_bf16 v[212:215], a[200:203], v[32:35], 0\n\t"
    "v_cvt_pk_bf16_f32 v229, v230, v231\n\t"
    "v_mul_f32 v220, v12, v220\n\t"
    "v_mfma_f32_16x16x32_bf16 v[212:215], a[204:207], v[36:39], v[212:215]\n\t"
    "ds_write_b64 v7, v[228:229] offset:32\n\t"
    "v_mul_f32 v221, v12, v221\n\t"
    "v_mul_f32 v222, v12, v222\n\t"
    "v_mul_f32 v223, v12, v223\n\t"
    "v_cvt_pk_bf16_f32 v220, v220, v221\n\t"
    "v_cvt_pk_bf16_f32 v221, v222, v223\n\t"
    "ds_write_b64 v7, v[220:221] offset:17632\n\t"
    "v_mfma_f32_16x16x32_bf16 v[216:219], a[208:211], v[32:35], 0\n\t"
    "v_mul_f32 v232, v13, v232\n\t"
    "v_mul_f32 v233, v13, v233\n\t"
    "v_mfma_f32_16x16x32_bf16 v[216:219], a[212:215], v[36:39], v[216:219]\n\t"
    "v_mul_f32 v234, v13, v234\n\t"
    "v_mul_f32 v235, v13, v235\n\t"
    "v_mfma_f32_16x16x32_bf16 v[220:223], a[216:219], v[32:35], 0\n\t"
    "v_cvt_pk_bf16_f32 v232, v232, v233\n\t"
    "v_cvt_pk_bf16_f32 v233, v234, v235\n\t"
    "v_exp_f32 v48, v48\n\t"
    "v_exp_f32 v49, v49\n\t"
    "v_exp_f32 v50, v50\n\t"
    "v_exp_f32 v51, v51\n\t"
    "v_mfma_f32_16x16x32_bf16 v[220:223], a[220:223], v[36:39], v[220:223]\n\t"
    "s_waitcnt lgkmcnt(15)\n\t"
    "ds_write_b64 v7, v[232:233] offset:64\n\t"
    "v_cvt_pk_bf16_f32 v40, v48, v49\n\t"
    "v_cvt_pk_bf16_f32 v41, v50, v51\n\t"
    "v_mfma_f32_16x16x32_bf16 v[48:51], a[96:99], a[0:3], 0\n\t"
    "v_mul_f32 v236, v13, v236\n\t"
    "v_mul_f32 v237, v13, v237\n\t"
    "v_mfma_f32_16x16x32_bf16 v[48:51], a[100:103], a[4:7], v[48:51]\n\t"
    "v_mul_f32 v238, v13, v238\n\t"
    "v_mul_f32 v239, v13, v239\n\t"
    "v_mfma_f32_16x16x32_bf16 v[48:51], a[104:107], a[8:11], v[48:51]\n\t"
    "v_exp_f32 v52, v52\n\t"
    "v_exp_f32 v53, v53\n\t"
    "v_exp_f32 v54, v54\n\t"
    "v_exp_f32 v55, v55\n\t"
    "v_mfma_f32_16x16x32_bf16 v[48:51], a[108:111], a[12:15], v[48:51]\n\t"
    "v_cvt_pk_bf16_f32 v236, v236, v237\n\t"
    "v_cvt_pk_bf16_f32 v237, v238, v239\n\t"
    "v_cvt_pk_bf16_f32 v42, v52, v53\n\t"
    "v_cvt_pk_bf16_f32 v43, v54, v55\n\t"
    "v_mfma_f32_16x16x32_bf16 v[52:55], a[112:115], a[0:3], 0\n\t"
    "ds_write_b64 v7, v[236:237] offset:96\n\t"
    "v_mfma_f32_16x16x32_bf16 v[52:55], a[116:119], a[4:7], v[52:55]\n\t"
    "v_mul_f32 v240, v13, v240\n\t"
    "v_mul_f32 v241, v13, v241\n\t"
    "v_mfma_f32_16x16x32_bf16 v[52:55], a[120:123], a[8:11], v[52:55]\n\t"
    "v_exp_f32 v56, v56\n\t"
    "v_exp_f32 v57, v57\n\t"
    "v_exp_f32 v58, v58\n\t"
    "v_exp_f32 v59, v59\n\t"
    "v_mfma_f32_16x16x32_bf16 v[52:55], a[124:127], a[12:15], v[52:55]\n\t"
    "s_waitcnt lgkmcnt(15)\n\t"
    "v_mul_f32 v242, v13, v242\n\t"
    "v_mul_f32 v243, v13, v243\n\t"
    "v_cvt_pk_bf16_f32 v44, v56, v57\n\t"
    "v_cvt_pk_bf16_f32 v45, v58, v59\n\t"
    "v_mfma_f32_16x16x32_bf16 v[56:59], a[128:131], a[0:3], 0\n\t"
    "s_waitcnt lgkmcnt(14)\n\t"
    "v_cvt_pk_bf16_f32 v240, v240, v241\n\t"
    "v_cvt_pk_bf16_f32 v241, v242, v243\n\t"
    "v_mfma_f32_16x16x32_bf16 v[56:59], a[132:135], a[4:7], v[56:59]\n\t"
    "s_waitcnt lgkmcnt(13)\n\t"
    "v_exp_f32 v60, v60\n\t"
    "v_mfma_f32_16x16x32_bf16 v[56:59], a[136:139], a[8:11], v[56:59]\n\t"
    "s_waitcnt lgkmcnt(12)\n\t"
    "ds_write_b64 v7, v[240:241] offset:128\n\t"
    "v_exp_f32 v61, v61\n\t"
    "v_exp_f32 v62, v62\n\t"
    "v_exp_f32 v63, v63\n\t"
    "v_mfma_f32_16x16x32_bf16 v[56:59], a[140:143], a[12:15], v[56:59]\n\t"
    "s_waitcnt lgkmcnt(12)\n\t"
    "v_mul_f32 v244, v13, v244\n\t"
    "v_mul_f32 v245, v13, v245\n\t"
    "v_cvt_pk_bf16_f32 v46, v60, v61\n\t"
    "v_cvt_pk_bf16_f32 v47, v62, v63\n\t"
    "v_mfma_f32_16x16x32_bf16 v[60:63], a[144:147], a[0:3], 0\n\t"
    "s_waitcnt lgkmcnt(11)\n\t"
    "v_mul_f32 v246, v13, v246\n\t"
    "v_mul_f32 v247, v13, v247\n\t"
    "v_mfma_f32_16x16x32_bf16 v[60:63], a[148:151], a[4:7], v[60:63]\n\t"
    "s_waitcnt lgkmcnt(10)\n\t"
    "v_cvt_pk_bf16_f32 v244, v244, v245\n\t"
    "v_cvt_pk_bf16_f32 v245, v246, v247\n\t"
    "v_mfma_f32_16x16x32_bf16 v[60:63], a[152:155], a[8:11], v[60:63]\n\t"
    "s_waitcnt lgkmcnt(9)\n\t"
    "ds_write_b64 v7, v[244:245] offset:160\n\t"
    "v_mfma_f32_16x16x32_bf16 v[60:63], a[156:159], a[12:15], v[60:63]\n\t"
    "v_mul_f32 v248, v13, v248\n\t"
    "v_mul_f32 v249, v13, v249\n\t"
    "v_smfmac_f32_16x16x64_bf16 a[244:247], a[248:251], v[40:47], v3\n\t"
    "v_mul_f32 v250, v13, v250\n\t"
    "v_mul_f32 v251, v13, v251\n\t"
    "v_mfma_f32_16x16x32_bf16 v[224:227], a[160:163], v[40:43], 0\n\t"
    "ds_read_b64_tr_b16 a[160:161], v1 offset:0\n\t"
    "v_cvt_pk_bf16_f32 v248, v248, v249\n\t"
    "v_mfma_f32_16x16x32_bf16 v[224:227], a[164:167], v[44:47], v[224:227]\n\t"
    "ds_read_b64_tr_b16 a[162:163], v1 offset:4096\n\t"
    "v_cvt_pk_bf16_f32 v249, v250, v251\n\t"
    "v_mfma_f32_16x16x32_bf16 v[228:231], a[168:171], v[40:43], 0\n\t"
    "ds_write_b64 v7, v[248:249] offset:192\n\t"
    "v_mfma_f32_16x16x32_bf16 v[228:231], a[172:175], v[44:47], v[228:231]\n\t"
    "ds_read_b64_tr_b16 a[164:165], v1 offset:8192\n\t"
    "v_xor_b32 v0, 0x4000, v0\n\t"
    "v_mfma_f32_16x16x32_bf16 v[232:235], a[176:179], v[40:43], 0\n\t"
    "v_exp_f32 v48, v48\n\t"
    "v_mfma_f32_16x16x32_bf16 v[232:235], a[180:183], v[44:47], v[232:235]\n\t"
    "ds_read_b64_tr_b16 a[166:167], v1 offset:12288\n\t"
    "v_mul_f32 v252, v13, v252\n\t"
    "v_mfma_f32_16x16x32_bf16 v[236:239], a[184:187], v[40:43], 0\n\t"
    "ds_read_b64_tr_b16 a[168:169], v2 offset:0\n\t"
    "v_mul_f32 v253, v13, v253\n\t"
    "v_mfma_f32_16x16x32_bf16 v[236:239], a[188:191], v[44:47], v[236:239]\n\t"
    "ds_read_b64_tr_b16 a[170:171], v2 offset:4096\n\t"
    "v_mul_f32 v254, v13, v254\n\t"
    "v_mfma_f32_16x16x32_bf16 v[240:243], a[192:195], v[40:43], 0\n\t"
    "ds_read_b64_tr_b16 a[172:173], v2 offset:8192\n\t"
    "v_mul_f32 v255, v13, v255\n\t"
    "v_mfma_f32_16x16x32_bf16 v[240:243], a[196:199], v[44:47], v[240:243]\n\t"
    "v_exp_f32 v49, v49\n\t"
    "v_mfma_f32_16x16x32_bf16 v[244:247], a[200:203], v[40:43], 0\n\t"
    "v_exp_f32 v50, v50\n\t"
    "v_mfma_f32_16x16x32_bf16 v[244:247], a[204:207], v[44:47], v[244:247]\n\t"
    "v_exp_f32 v51, v51\n\t"
    "v_cvt_pk_bf16_f32 v252, v252, v253\n\t"
    "v_cvt_pk_bf16_f32 v253, v254, v255\n\t"
    "ds_write_b64 v7, v[252:253] offset:224\n\t"
    "v_mfma_f32_16x16x32_bf16 v[248:251], a[208:211], v[40:43], 0\n\t"
    "ds_read_b64_tr_b16 a[174:175], v2 offset:12288\n\t"
    "v_cvt_pk_bf16_f32 v32, v48, v49\n\t"
    "v_mfma_f32_16x16x32_bf16 v[248:251], a[212:215], v[44:47], v[248:251]\n\t"
    "ds_read_b64_tr_b16 a[176:177], v1 offset:512\n\t"
    "v_cvt_pk_bf16_f32 v33, v50, v51\n\t"
    "v_mfma_f32_16x16x32_bf16 v[252:255], a[216:219], v[40:43], 0\n\t"
    "v_exp_f32 v52, v52\n\t"
    "v_mfma_f32_16x16x32_bf16 v[252:255], a[220:223], v[44:47], v[252:255]\n\t"
    "v_exp_f32 v53, v53\n\t"
    "v_mfma_f32_16x16x32_bf16 v[48:51], a[96:99], a[16:19], 0\n\t"
    "v_exp_f32 v54, v54\n\t"
    "v_mfma_f32_16x16x32_bf16 v[48:51], a[100:103], a[20:23], v[48:51]\n\t"
    "v_exp_f32 v55, v55\n\t"
    "v_mfma_f32_16x16x32_bf16 v[48:51], a[104:107], a[24:27], v[48:51]\n\t"
    "ds_read_b64_tr_b16 a[178:179], v1 offset:4608\n\t"
    "v_cvt_pk_bf16_f32 v34, v52, v53\n\t"
    "v_mfma_f32_16x16x32_bf16 v[48:51], a[108:111], a[28:31], v[48:51]\n\t"
    "ds_read_b64_tr_b16 a[180:181], v1 offset:8704\n\t"
    "v_cvt_pk_bf16_f32 v35, v54, v55\n\t"
    "v_mfma_f32_16x16x32_bf16 v[52:55], a[112:115], a[16:19], 0\n\t"
    "v_exp_f32 v56, v56\n\t"
    "v_mfma_f32_16x16x32_bf16 v[52:55], a[116:119], a[20:23], v[52:55]\n\t"
    "v_exp_f32 v57, v57\n\t"
    "v_mfma_f32_16x16x32_bf16 v[52:55], a[120:123], a[24:27], v[52:55]\n\t"
    "v_exp_f32 v58, v58\n\t"
    "v_exp_f32 v59, v59\n\t"
    "v_mfma_f32_16x16x32_bf16 v[52:55], a[124:127], a[28:31], v[52:55]\n\t"
    "ds_read_b64_tr_b16 a[182:183], v1 offset:12800\n\t"
    "v_cvt_pk_bf16_f32 v36, v56, v57\n\t"
    "v_cvt_pk_bf16_f32 v37, v58, v59\n\t"
    "v_mfma_f32_16x16x32_bf16 v[56:59], a[128:131], a[16:19], 0\n\t"
    "v_exp_f32 v60, v60\n\t"
    "v_mfma_f32_16x16x32_bf16 v[56:59], a[132:135], a[20:23], v[56:59]\n\t"
    "v_exp_f32 v61, v61\n\t"
    "v_mfma_f32_16x16x32_bf16 v[56:59], a[136:139], a[24:27], v[56:59]\n\t"
    "v_exp_f32 v62, v62\n\t"
    "v_exp_f32 v63, v63\n\t"
    "v_mfma_f32_16x16x32_bf16 v[56:59], a[140:143], a[28:31], v[56:59]\n\t"
    "ds_read_b64_tr_b16 a[184:185], v2 offset:512\n\t"
    "v_cvt_pk_bf16_f32 v38, v60, v61\n\t"
    "v_cvt_pk_bf16_f32 v39, v62, v63\n\t"
    "v_mfma_f32_16x16x32_bf16 v[60:63], a[144:147], a[16:19], 0\n\t"
    "ds_read_b64_tr_b16 a[186:187], v2 offset:4608\n\t"
    "ds_read_b64_tr_b16 a[188:189], v2 offset:8704\n\t"
    "v_mfma_f32_16x16x32_bf16 v[60:63], a[148:151], a[20:23], v[60:63]\n\t"
    "ds_read_b64_tr_b16 a[190:191], v2 offset:12800\n\t"
    "ds_read_b64_tr_b16 a[192:193], v1 offset:1024\n\t"
    "v_mfma_f32_16x16x32_bf16 v[60:63], a[152:155], a[24:27], v[60:63]\n\t"
    "v_exp_f32 v48, v48\n\t"
    "v_mfma_f32_16x16x32_bf16 v[60:63], a[156:159], a[28:31], v[60:63]\n\t"
    "ds_read_b64_tr_b16 a[194:195], v1 offset:5120\n\t"
    "ds_read_b64_tr_b16 a[196:197], v1 offset:9216\n\t"
    "v_smfmac_f32_16x16x64_bf16 a[224:227], a[248:251], v[32:39], v3\n\t"
    "s_waitcnt lgkmcnt(15)\n\t"
    "v_exp_f32 v49, v49\n\t"
    "v_mfma_f32_16x16x32_bf16 v[64:67], a[160:163], v[32:35], v[64:67]\n\t"
    "ds_read_b128 v[16:19], v8 offset:0\n\t"
    "ds_read_b64_tr_b16 a[198:199], v1 offset:13312\n\t"
    "v_mfma_f32_16x16x32_bf16 v[64:67], a[164:167], v[36:39], v[64:67]\n\t"
    "s_waitcnt lgkmcnt(15)\n\t"
    "ds_read_b64_tr_b16 a[200:201], v2 offset:1024\n\t"
    "ds_read_b64_tr_b16 a[202:203], v2 offset:5120\n\t"
    "v_mfma_f32_16x16x32_bf16 v[68:71], a[168:171], v[32:35], v[68:71]\n\t"
    "s_waitcnt lgkmcnt(15)\n\t"
    "ds_read_b128 v[20:23], v8 offset:1088\n\t"
    "ds_read_b64_tr_b16 a[204:205], v2 offset:9216\n\t"
    "v_mfma_f32_16x16x32_bf16 v[68:71], a[172:175], v[36:39], v[68:71]\n\t"
    "s_waitcnt lgkmcnt(15)\n\t"
    "v_exp_f32 v50, v50\n\t"
    "v_mfma_f32_16x16x32_bf16 v[72:75], a[176:179], v[32:35], v[72:75]\n\t"
    "s_waitcnt lgkmcnt(13)\n\t"
    "v_exp_f32 v51, v51\n\t"
    "ds_read_b64_tr_b16 a[206:207], v2 offset:13312\n\t"
    "v_mfma_f32_16x16x32_bf16 v[72:75], a[180:183], v[36:39], v[72:75]\n\t"
    "s_waitcnt lgkmcnt(12)\n\t"
    "ds_read_b64_tr_b16 a[208:209], v1 offset:1536\n\t"
    "v_cvt_pk_bf16_f32 v40, v48, v49\n\t"
    "ds_read_b64_tr_b16 a[210:211], v1 offset:5632\n\t"
    "v_mfma_f32_16x16x32_bf16 v[76:79], a[184:187], v[32:35], v[76:79]\n\t"
    "s_waitcnt lgkmcnt(12)\n\t"
    "ds_read_b64_tr_b16 a[212:213], v1 offset:9728\n\t"
    "v_cvt_pk_bf16_f32 v41, v50, v51\n\t"
    "ds_read_b64_tr_b16 a[214:215], v1 offset:13824\n\t"
    "v_mfma_f32_16x16x32_bf16 v[76:79], a[188:191], v[36:39], v[76:79]\n\t"
    "s_waitcnt lgkmcnt(12)\n\t"
    "v_exp_f32 v52, v52\n\t"
    "ds_read_b64_tr_b16 a[216:217], v2 offset:1536\n\t"
    "ds_read_b64_tr_b16 a[218:219], v2 offset:5632\n\t"
    "v_mfma_f32_16x16x32_bf16 v[80:83], a[192:195], v[32:35], v[80:83]\n\t"
    "s_waitcnt lgkmcnt(11)\n\t"
    "v_exp_f32 v53, v53\n\t"
    "ds_read_b64_tr_b16 a[220:221], v2 offset:9728\n\t"
    "ds_read_b64_tr_b16 a[222:223], v2 offset:13824\n\t"
    "v_cmp_lt_u32 vcc, s55, v10\n\t"
    "v_mfma_f32_16x16x32_bf16 v[80:83], a[196:199], v[36:39], v[80:83]\n\t"
    "s_waitcnt lgkmcnt(11)\n\t"
    "v_exp_f32 v54, v54\n\t"
    "v_mfma_f32_16x16x32_bf16 v[84:87], a[200:203], v[32:35], v[84:87]\n\t"
    "s_waitcnt lgkmcnt(8)\n\t"
    "buffer_store_dwordx4 v[16:19], v9, s[48:51], s64 offen offset:0 sc1\n\t"
    "v_mov_b32 v10, 0\n\t"
    "v_mfma_f32_16x16x32_bf16 v[84:87], a[204:207], v[36:39], v[84:87]\n\t"
    "s_waitcnt lgkmcnt(6)\n\t"
    "v_exp_f32 v55, v55\n\t"
    "v_mfma_f32_16x16x32_bf16 v[88:91], a[208:211], v[32:35], v[88:91]\n\t"
    "s_waitcnt lgkmcnt(4)\n\t"
    "ds_read_b128 v[16:19], v8 offset:2176\n\t"
    "v_cvt_pk_bf16_f32 v42, v52, v53\n\t"
    "v_mfma_f32_16x16x32_bf16 v[88:91], a[212:215], v[36:39], v[88:91]\n\t"
    "s_waitcnt lgkmcnt(3)\n\t"
    "buffer_store_dwordx4 v[20:23], v9, s[48:51], s64 offen offset:1024 sc1\n\t"
    "v_cvt_pk_bf16_f32 v43, v54, v55\n\t"
    "v_mfma_f32_16x16x32_bf16 v[92:95], a[216:219], v[32:35], v[92:95]\n\t"
    "s_waitcnt lgkmcnt(1)\n\t"
    "s_or_b32 s65, vcc_lo, vcc_hi\n\t"
    "v_exp_f32 v56, v56\n\t"
    "v_exp_f32 v57, v57\n\t"
    "v_exp_f32 v58, v58\n\t"
    "v_exp_f32 v59, v59\n\t"
    "ds_read_b128 v[20:23], v8 offset:3264\n\t"
    "s_waitcnt lgkmcnt(1)\n\t"
    "buffer_store_dwordx4 v[16:19], v9, s[48:51], s64 offen offset:2048 sc1\n\t"
    "s_waitcnt lgkmcnt(0)\n\t"
    "buffer_store_dwordx4 v[20:23], v9, s[48:51], s64 offen offset:3072 sc1\n\t"
    "v_mfma_f32_16x16x32_bf16 v[92:95], a[220:223], v[36:39], v[92:95]\n\t"
    "s_add_u32 s62, s62, 4096\n\t"
    "v_cvt_pk_bf16_f32 v44, v56, v57\n\t"
    "v_xor_b32 v1, 0x4000, v1\n\t"
    "v_cvt_pk_bf16_f32 v45, v58, v59\n\t"
    "v_xor_b32 v2, 0x4000, v2\n\t"
    "v_add_u32 v8, 4352, v8\n\t"
    ".p2align 4\n\t"
    "L_L1:\n\t"
    "v_mfma_f32_16x16x32_bf16 v[48:51], a[96:99], a[32:35], 0\n\t"
    "s_waitcnt vmcnt(0) lgkmcnt(0)\n\t"
    "s_barrier\n\t"
    "v_exp_f32 v60, v60\n\t"
    "v_mfma_f32_16x16x32_bf16 v[48:51], a[100:103], a[36:39], v[48:51]\n\t"
    "s_cmp_gt_u32 s57, 2\n\t"
    "s_cselect_b32 s50, s67, 0\n\t"
    "v_exp_f32 v61, v61\n\t"
    "v_mfma_f32_16x16x32_bf16 v[48:51], a[104:107], a[40:43], v[48:51]\n\t"
    "s_add_u32 m0, s59, 0\n\t"
    "v_exp_f32 v62, v62\n\t"
    "v_mfma_f32_16x16x32_bf16 v[48:51], a[108:111], a[44:47], v[48:51]\n\t"
    "v_exp_f32 v63, v63\n\t"
    "v_mfma_f32_16x16x32_bf16 v[52:55], a[112:115], a[32:35], 0\n\t"
    "buffer_load_dwordx4 v4, s[36:39], s58 offen offset:0 lds\n\t"
    "v_cvt_pk_bf16_f32 v46, v60, v61\n\t"
    "v_mfma_f32_16x16x32_bf16 v[52:55], a[116:119], a[36:39], v[52:55]\n\t"
    "v_cvt_pk_bf16_f32 v47, v62, v63\n\t"
    "v_mfma_f32_16x16x32_bf16 v[52:55], a[120:123], a[40:43], v[52:55]\n\t"
    "v_mfma_f32_16x16x32_bf16 v[52:55], a[124:127], a[44:47], v[52:55]\n\t"
    "v_exp_f32 v48, v48\n\t"
    "v_mfma_f32_16x16x32_bf16 v[56:59], a[128:131], a[32:35], 0\n\t"
    "v_exp_f32 v49, v49\n\t"
    "v_mfma_f32_16x16x32_bf16 v[56:59], a[132:135], a[36:39], v[56:59]\n\t"
    "s_add_u32 m0, s59, 960\n\t"
    "v_exp_f32 v50, v50\n\t"
    "v_mfma_f32_16x16x32_bf16 v[56:59], a[136:139], a[40:43], v[56:59]\n\t"
    "v_exp_f32 v51, v51\n\t"
    "v_mfma_f32_16x16x32_bf16 v[56:59], a[140:143], a[44:47], v[56:59]\n\t"
    "buffer_load_dwordx4 v4, s[36:39], s58 offen offset:64 lds\n\t"
    "v_cvt_pk_bf16_f32 v32, v48, v49\n\t"
    "v_mfma_f32_16x16x32_bf16 v[60:63], a[144:147], a[32:35], 0\n\t"
    "v_cvt_pk_bf16_f32 v33, v50, v51\n\t"
    "v_mfma_f32_16x16x32_bf16 v[60:63], a[148:151], a[36:39], v[60:63]\n\t"
    "v_exp_f32 v52, v52\n\t"
    "v_mfma_f32_16x16x32_bf16 v[60:63], a[152:155], a[40:43], v[60:63]\n\t"
    "v_exp_f32 v53, v53\n\t"
    "v_mfma_f32_16x16x32_bf16 v[60:63], a[156:159], a[44:47], v[60:63]\n\t"
    "v_exp_f32 v54, v54\n\t"
    "v_smfmac_f32_16x16x64_bf16 a[228:231], a[248:251], v[40:47], v3\n\t"
    "s_add_u32 m0, s59, 1920\n\t"
    "v_exp_f32 v55, v55\n\t"
    "v_mfma_f32_16x16x32_bf16 v[96:99], a[160:163], v[40:43], v[96:99]\n\t"
    "buffer_load_dwordx4 v4, s[36:39], s58 offen offset:128 lds\n\t"
    "v_cvt_pk_bf16_f32 v34, v52, v53\n\t"
    "v_mfma_f32_16x16x32_bf16 v[96:99], a[164:167], v[44:47], v[96:99]\n\t"
    "v_cvt_pk_bf16_f32 v35, v54, v55\n\t"
    "v_mfma_f32_16x16x32_bf16 v[100:103], a[168:171], v[40:43], v[100:103]\n\t"
    "v_exp_f32 v56, v56\n\t"
    "v_mfma_f32_16x16x32_bf16 v[100:103], a[172:175], v[44:47], v[100:103]\n\t"
    "v_exp_f32 v57, v57\n\t"
    "v_mfma_f32_16x16x32_bf16 v[104:107], a[176:179], v[40:43], v[104:107]\n\t"
    "v_exp_f32 v58, v58\n\t"
    "v_mfma_f32_16x16x32_bf16 v[104:107], a[180:183], v[44:47], v[104:107]\n\t"
    "v_exp_f32 v59, v59\n\t"
    "v_mfma_f32_16x16x32_bf16 v[108:111], a[184:187], v[40:43], v[108:111]\n\t"
    "s_add_u32 m0, s59, 2880\n\t"
    "v_cvt_pk_bf16_f32 v36, v56, v57\n\t"
    "v_cvt_pk_bf16_f32 v37, v58, v59\n\t"
    "v_mfma_f32_16x16x32_bf16 v[108:111], a[188:191], v[44:47], v[108:111]\n\t"
    "v_exp_f32 v60, v60\n\t"
    "v_mfma_f32_16x16x32_bf16 v[112:115], a[192:195], v[40:43], v[112:115]\n\t"
    "v_exp_f32 v61, v61\n\t"
    "v_mfma_f32_16x16x32_bf16 v[112:115], a[196:199], v[44:47], v[112:115]\n\t"
    "v_exp_f32 v62, v62\n\t"
    "v_mfma_f32_16x16x32_bf16 v[116:119], a[200:203], v[40:43], v[116:119]\n\t"
    "v_exp_f32 v63, v63\n\t"
    "v_mfma_f32_16x16x32_bf16 v[116:119], a[204:207], v[44:47], v[116:119]\n\t"
    "buffer_load_dwordx4 v4, s[36:39], s58 offen offset:192 lds\n\t"
    "v_cvt_pk_bf16_f32 v38, v60, v61\n\t"
    "v_mfma_f32_16x16x32_bf16 v[120:123], a[208:211], v[40:43], v[120:123]\n\t"
    "v_cvt_pk_bf16_f32 v39, v62, v63\n\t"
    "v_mfma_f32_16x16x32_bf16 v[120:123], a[212:215], v[44:47], v[120:123]\n\t"
    "s_add_u32 m0, s59, 32768\n\t"
    "v_mfma_f32_16x16x32_bf16 v[124:127], a[216:219], v[40:43], v[124:127]\n\t"
    "buffer_load_dwordx4 v5, s[40:43], s58 offen offset:0 lds\n\t"
    "v_mfma_f32_16x16x32_bf16 v[124:127], a[220:223], v[44:47], v[124:127]\n\t"
    "v_mfma_f32_16x16x32_bf16 v[48:51], a[96:99], a[48:51], 0\n\t"
    "v_mfma_f32_16x16x32_bf16 v[48:51], a[100:103], a[52:55], v[48:51]\n\t"
    "v_mfma_f32_16x16x32_bf16 v[48:51], a[104:107], a[56:59], v[48:51]\n\t"
    "v_mfma_f32_16x16x32_bf16 v[48:51], a[108:111], a[60:63], v[48:51]\n\t"
    "v_mfma_f32_16x16x32_bf16 v[52:55], a[112:115], a[48:51], 0\n\t"
    "v_mfma_f32_16x16x32_bf16 v[52:55], a[116:119], a[52:55], v[52:55]\n\t"
    "buffer_load_dwordx4 v5, s[40:43], s58 offen offset:2048 lds\n\t"
    "v_mfma_f32_16x16x32_bf16 v[52:55], a[120:123], a[56:59], v[52:55]\n\t"
    "v_mfma_f32_16x16x32_bf16 v[52:55], a[124:127], a[60:63], v[52:55]\n\t"
    "v_mfma_f32_16x16x32_bf16 v[56:59], a[128:131], a[48:51], 0\n\t"
    "v_mfma_f32_16x16x32_bf16 v[56:59], a[132:135], a[52:55], v[56:59]\n\t"
    "v_mfma_f32_16x16x32_bf16 v[56:59], a[136:139], a[56:59], v[56:59]\n\t"
    "v_exp_f32 v48, v48\n\t"
    "v_mfma_f32_16x16x32_bf16 v[56:59], a[140:143], a[60:63], v[56:59]\n\t"
    "s_add_u32 m0, s59, 33664\n\t"
    "v_exp_f32 v49, v49\n\t"
    "v_mfma_f32_16x16x32_bf16 v[60:63], a[144:147], a[48:51], 0\n\t"
    "v_exp_f32 v50, v50\n\t"
    "v_mfma_f32_16x16x32_bf16 v[60:63], a[148:151], a[52:55], v[60:63]\n\t"
    "v_exp_f32 v51, v51\n\t"
    "v_mfma_f32_16x16x32_bf16 v[60:63], a[152:155], a[56:59], v[60:63]\n\t"
    "buffer_load_dwordx4 v5, s[40:43], s58 offen offset:128 lds\n\t"
    "v_cvt_pk_bf16_f32 v40, v48, v49\n\t"
    "v_mfma_f32_16x16x32_bf16 v[60:63], a[156:159], a[60:63], v[60:63]\n\t"
    "v_cvt_pk_bf16_f32 v41, v50, v51\n\t"
    "v_smfmac_f32_16x16x64_bf16 a[232:235], a[248:251], v[32:39], v3\n\t"
    "v_exp_f32 v52, v52\n\t"
    "v_mfma_f32_16x16x32_bf16 v[128:131], a[160:163], v[32:35], v[128:131]\n\t"
    "v_exp_f32 v53, v53\n\t"
    "v_mfma_f32_16x16x32_bf16 v[128:131], a[164:167], v[36:39], v[128:131]\n\t"
    "v_exp_f32 v54, v54\n\t"
    "v_mfma_f32_16x16x32_bf16 v[132:135], a[168:171], v[32:35], v[132:135]\n\t"
    "v_exp_f32 v55, v55\n\t"
    "v_mfma_f32_16x16x32_bf16 v[132:135], a[172:175], v[36:39], v[132:135]\n\t"
    "buffer_load_dwordx4 v5, s[40:43], s58 offen offset:2176 lds\n\t"
    "v_cvt_pk_bf16_f32 v42, v52, v53\n\t"
    "v_mfma_f32_16x16x32_bf16 v[136:139], a[176:179], v[32:35], v[136:139]\n\t"
    "ds_read_b128 v[16:19], v8 offset:0\n\t"
    "v_cvt_pk_bf16_f32 v43, v54, v55\n\t"
    "v_mfma_f32_16x16x32_bf16 v[136:139], a[180:183], v[36:39], v[136:139]\n\t"
    "v_exp_f32 v56, v56\n\t"
    "v_mfma_f32_16x16x32_bf16 v[140:143], a[184:187], v[32:35], v[140:143]\n\t"
    "v_exp_f32 v57, v57\n\t"
    "v_mfma_f32_16x16x32_bf16 v[140:143], a[188:191], v[36:39], v[140:143]\n\t"
    "v_exp_f32 v58, v58\n\t"
    "v_mfma_f32_16x16x32_bf16 v[144:147], a[192:195], v[32:35], v[144:147]\n\t"
    "v_exp_f32 v59, v59\n\t"
    "v_mfma_f32_16x16x32_bf16 v[144:147], a[196:199], v[36:39], v[144:147]\n\t"
    "ds_read_b128 v[20:23], v8 offset:1088\n\t"
    "v_cvt_pk_bf16_f32 v44, v56, v57\n\t"
    "v_mfma_f32_16x16x32_bf16 v[148:151], a[200:203], v[32:35], v[148:151]\n\t"
    "s_sub_u32 s58, s58, 16384\n\t"
    "s_waitcnt lgkmcnt(1)\n\t"
    "buffer_store_dwordx4 v[16:19], v9, s[48:51], s62 offen offset:0 sc1\n\t"
    "v_cvt_pk_bf16_f32 v45, v58, v59\n\t"
    "v_mfma_f32_16x16x32_bf16 v[148:151], a[204:207], v[36:39], v[148:151]\n\t"
    "s_lshl_b32 s68, s56, 12\n\t"
    "v_exp_f32 v60, v60\n\t"
    "v_mfma_f32_16x16x32_bf16 v[152:155], a[208:211], v[32:35], v[152:155]\n\t"
    "s_max_i32 s58, s58, s68\n\t"
    "v_exp_f32 v61, v61\n\t"
    "v_mfma_f32_16x16x32_bf16 v[152:155], a[212:215], v[36:39], v[152:155]\n\t"
    "s_xor_b32 s59, s59, 0x4000\n\t"
    "v_exp_f32 v62, v62\n\t"
    "v_mfma_f32_16x16x32_bf16 v[156:159], a[216:219], v[32:35], v[156:159]\n\t"
    "v_exp_f32 v63, v63\n\t"
    "v_mfma_f32_16x16x32_bf16 v[156:159], a[220:223], v[36:39], v[156:159]\n\t"
    "ds_read_b128 v[16:19], v8 offset:2176\n\t"
    "v_cvt_pk_bf16_f32 v46, v60, v61\n\t"
    "v_mfma_f32_16x16x32_bf16 v[48:51], a[96:99], a[64:67], 0\n\t"
    "v_cvt_pk_bf16_f32 v47, v62, v63\n\t"
    "v_mfma_f32_16x16x32_bf16 v[48:51], a[100:103], a[68:71], v[48:51]\n\t"
    "v_mfma_f32_16x16x32_bf16 v[48:51], a[104:107], a[72:75], v[48:51]\n\t"
    "v_mfma_f32_16x16x32_bf16 v[48:51], a[108:111], a[76:79], v[48:51]\n\t"
    "s_waitcnt lgkmcnt(1)\n\t"
    "buffer_store_dwordx4 v[20:23], v9, s[48:51], s62 offen offset:1024 sc1\n\t"
    "v_mfma_f32_16x16x32_bf16 v[52:55], a[112:115], a[64:67], 0\n\t"
    "v_mfma_f32_16x16x32_bf16 v[52:55], a[116:119], a[68:71], v[52:55]\n\t"
    "v_mfma_f32_16x16x32_bf16 v[52:55], a[120:123], a[72:75], v[52:55]\n\t"
    "v_mfma_f32_16x16x32_bf16 v[52:55], a[124:127], a[76:79], v[52:55]\n\t"
    "ds_read_b128 v[20:23], v8 offset:3264\n\t"
    "v_mfma_f32_16x16x32_bf16 v[56:59], a[128:131], a[64:67], 0\n\t"
    "v_exp_f32 v48, v48\n\t"
    "v_mfma_f32_16x16x32_bf16 v[56:59], a[132:135], a[68:71], v[56:59]\n\t"
    "v_exp_f32 v49, v49\n\t"
    "v_mfma_f32_16x16x32_bf16 v[56:59], a[136:139], a[72:75], v[56:59]\n\t"
    "v_exp_f32 v50, v50\n\t"
    "v_mfma_f32_16x16x32_bf16 v[56:59], a[140:143], a[76:79], v[56:59]\n\t"
    "v_exp_f32 v51, v51\n\t"
    "v_mfma_f32_16x16x32_bf16 v[60:63], a[144:147], a[64:67], 0\n\t"
    "v_cvt_pk_bf16_f32 v32, v48, v49\n\t"
    "v_cvt_pk_bf16_f32 v33, v50, v51\n\t"
    "v_mfma_f32_16x16x32_bf16 v[60:63], a[148:151], a[68:71], v[60:63]\n\t"
    "v_exp_f32 v52, v52\n\t"
    "v_mfma_f32_16x16x32_bf16 v[60:63], a[152:155], a[72:75], v[60:63]\n\t"
    "v_exp_f32 v53, v53\n\t"
    "v_mfma_f32_16x16x32_bf16 v[60:63], a[156:159], a[76:79], v[60:63]\n\t"
    "v_exp_f32 v54, v54\n\t"
    "v_smfmac_f32_16x16x64_bf16 a[236:239], a[248:251], v[40:47], v3\n\t"
    "v_exp_f32 v55, v55\n\t"
    "v_mfma_f32_16x16x32_bf16 v[160:163], a[160:163], v[40:43], v[160:163]\n\t"
    "s_waitcnt lgkmcnt(1)\n\t"
    "buffer_store_dwordx4 v[16:19], v9, s[48:51], s62 offen offset:2048 sc1\n\t"
    "v_cvt_pk_bf16_f32 v34, v52, v53\n\t"
    "v_mfma_f32_16x16x32_bf16 v[160:163], a[164:167], v[44:47], v[160:163]\n\t"
    "v_cvt_pk_bf16_f32 v35, v54, v55\n\t"
    "v_mfma_f32_16x16x32_bf16 v[164:167], a[168:171], v[40:43], v[164:167]\n\t"
    "v_exp_f32 v56, v56\n\t"
    "v_mfma_f32_16x16x32_bf16 v[164:167], a[172:175], v[44:47], v[164:167]\n\t"
    "v_exp_f32 v57, v57\n\t"
    "v_mfma_f32_16x16x32_bf16 v[168:171], a[176:179], v[40:43], v[168:171]\n\t"
    "v_exp_f32 v58, v58\n\t"
    "v_mfma_f32_16x16x32_bf16 v[168:171], a[180:183], v[44:47], v[168:171]\n\t"
    "v_exp_f32 v59, v59\n\t"
    "v_mfma_f32_16x16x32_bf16 v[172:175], a[184:187], v[40:43], v[172:175]\n\t"
    "s_waitcnt lgkmcnt(0)\n\t"
    "buffer_store_dwordx4 v[20:23], v9, s[48:51], s62 offen offset:3072 sc1\n\t"
    "v_cvt_pk_bf16_f32 v36, v56, v57\n\t"
    "v_mfma_f32_16x16x32_bf16 v[172:175], a[188:191], v[44:47], v[172:175]\n\t"
    "s_add_u32 s62, s62, 4096\n\t"
    "v_cvt_pk_bf16_f32 v37, v58, v59\n\t"
    "v_add_u32 v8, 4352, v8\n\t"
    "v_mfma_f32_16x16x32_bf16 v[176:179], a[192:195], v[40:43], v[176:179]\n\t"
    "s_sub_u32 s57, s57, 1\n\t"
    "v_exp_f32 v60, v60\n\t"
    "v_mfma_f32_16x16x32_bf16 v[176:179], a[196:199], v[44:47], v[176:179]\n\t"
    "v_exp_f32 v61, v61\n\t"
    "v_mfma_f32_16x16x32_bf16 v[180:183], a[200:203], v[40:43], v[180:183]\n\t"
    "v_exp_f32 v62, v62\n\t"
    "v_mfma_f32_16x16x32_bf16 v[180:183], a[204:207], v[44:47], v[180:183]\n\t"
    "v_exp_f32 v63, v63\n\t"
    "v_mfma_f32_16x16x32_bf16 v[184:187], a[208:211], v[40:43], v[184:187]\n\t"
    "v_cvt_pk_bf16_f32 v38, v60, v61\n\t"
    "v_cvt_pk_bf16_f32 v39, v62, v63\n\t"
    "v_mfma_f32_16x16x32_bf16 v[184:187], a[212:215], v[44:47], v[184:187]\n\t"
    "v_mfma_f32_16x16x32_bf16 v[188:191], a[216:219], v[40:43], v[188:191]\n\t"
    "v_mfma_f32_16x16x32_bf16 v[188:191], a[220:223], v[44:47], v[188:191]\n\t"
    "v_mfma_f32_16x16x32_bf16 v[48:51], a[96:99], a[80:83], 0\n\t"
    "ds_read_b128 a[96:99], v0 offset:0\n\t"
    "v_mfma_f32_16x16x32_bf16 v[48:51], a[100:103], a[84:87], v[48:51]\n\t"
    "ds_read_b128 a[100:103], v0 offset:1024\n\t"
    "v_mfma_f32_16x16x32_bf16 v[48:51], a[104:107], a[88:91], v[48:51]\n\t"
    "ds_read_b128 a[104:107], v0 offset:2048\n\t"
    "v_mfma_f32_16x16x32_bf16 v[48:51], a[108:111], a[92:95], v[48:51]\n\t"
    "ds_read_b128 a[108:111], v0 offset:3072\n\t"
    "v_mfma_f32_16x16x32_bf16 v[52:55], a[112:115], a[80:83], 0\n\t"
    "ds_read_b128 a[112:115], v0 offset:4096\n\t"
    "v_mfma_f32_16x16x32_bf16 v[52:55], a[116:119], a[84:87], v[52:55]\n\t"
    "ds_read_b128 a[116:119], v0 offset:5120\n\t"
    "v_mfma_f32_16x16x32_bf16 v[52:55], a[120:123], a[88:91], v[52:55]\n\t"
    "ds_read_b128 a[120:123], v0 offset:6144\n\t"
    "v_mfma_f32_16x16x32_bf16 v[52:55], a[124:127], a[92:95], v[52:55]\n\t"
    "v_exp_f32 v48, v48\n\t"
    "v_mfma_f32_16x16x32_bf16 v[56:59], a[128:131], a[80:83], 0\n\t"
    "v_exp_f32 v49, v49\n\t"
    "v_mfma_f32_16x16x32_bf16 v[56:59], a[132:135], a[84:87], v[56:59]\n\t"
    "v_exp_f32 v50, v50\n\t"
    "v_mfma_f32_16x16x32_bf16 v[56:59], a[136:139], a[88:91], v[56:59]\n\t"
    "v_exp_f32 v51, v51\n\t"
    "v_mfma_f32_16x16x32_bf16 v[56:59], a[140:143], a[92:95], v[56:59]\n\t"
    "ds_read_b128 a[124:127], v0 offset:7168\n\t"
    "v_cvt_pk_bf16_f32 v40, v48, v49\n\t"
    "v_mfma_f32_16x16x32_bf16 v[60:63], a[144:147], a[80:83], 0\n\t"
    "ds_read_b128 a[128:131], v0 offset:8192\n\t"
    "v_cvt_pk_bf16_f32 v41, v50, v51\n\t"
    "v_mfma_f32_16x16x32_bf16 v[60:63], a[148:151], a[84:87], v[60:63]\n\t"
    "v_exp_f32 v52, v52\n\t"
    "v_mfma_f32_16x16x32_bf16 v[60:63], a[152:155], a[88:91], v[60:63]\n\t"
    "v_exp_f32 v53, v53\n\t"
    "v_mfma_f32_16x16x32_bf16 v[60:63], a[156:159], a[92:95], v[60:63]\n\t"
    "v_exp_f32 v54, v54\n\t"
    "v_smfmac_f32_16x16x64_bf16 a[240:243], a[248:251], v[32:39], v3\n\t"
    "v_exp_f32 v55, v55\n\t"
    "v_mfma_f32_16x16x32_bf16 v[192:195], a[160:163], v[32:35], v[192:195]\n\t"
    "ds_read_b128 a[132:135], v0 offset:9216\n\t"
    "v_cvt_pk_bf16_f32 v42, v52, v53\n\t"
    "v_mfma_f32_16x16x32_bf16 v[192:195], a[164:167], v[36:39], v[192:195]\n\t"
    "ds_read_b128 a[136:139], v0 offset:10240\n\t"
    "v_cvt_pk_bf16_f32 v43, v54, v55\n\t"
    "v_mfma_f32_16x16x32_bf16 v[196:199], a[168:171], v[32:35], v[196:199]\n\t"
    "v_exp_f32 v56, v56\n\t"
    "v_mfma_f32_16x16x32_bf16 v[196:199], a[172:175], v[36:39], v[196:199]\n\t"
    "v_exp_f32 v57, v57\n\t"
    "v_mfma_f32_16x16x32_bf16 v[200:203], a[176:179], v[32:35], v[200:203]\n\t"
    "v_exp_f32 v58, v58\n\t"
    "v_mfma_f32_16x16x32_bf16 v[200:203], a[180:183], v[36:39], v[200:203]\n\t"
    "ds_read_b128 a[140:143], v0 offset:11264\n\t"
    "ds_read_b128 a[144:147], v0 offset:12288\n\t"
    "v_mfma_f32_16x16x32_bf16 v[204:207], a[184:187], v[32:35], v[204:207]\n\t"
    "v_exp_f32 v59, v59\n\t"
    "v_mfma_f32_16x16x32_bf16 v[204:207], a[188:191], v[36:39], v[204:207]\n\t"
    "ds_read_b128 a[148:151], v0 offset:13312\n\t"
    "v_cvt_pk_bf16_f32 v44, v56, v57\n\t"
    "v_mfma_f32_16x16x32_bf16 v[208:211], a[192:195], v[32:35], v[208:211]\n\t"
    "ds_read_b128 a[152:155], v0 offset:14336\n\t"
    "v_cvt_pk_bf16_f32 v45, v58, v59\n\t"
    "v_mfma_f32_16x16x32_bf16 v[208:211], a[196:199], v[36:39], v[208:211]\n\t"
    "v_exp_f32 v60, v60\n\t"
    "v_mfma_f32_16x16x32_bf16 v[212:215], a[200:203], v[32:35], v[212:215]\n\t"
    "v_exp_f32 v61, v61\n\t"
    "v_mfma_f32_16x16x32_bf16 v[212:215], a[204:207], v[36:39], v[212:215]\n\t"
    "ds_read_b128 a[156:159], v0 offset:15360\n\t"
    "v_xor_b32 v0, 0x4000, v0\n\t"
    "v_mfma_f32_16x16x32_bf16 v[216:219], a[208:211], v[32:35], v[216:219]\n\t"
    "v_exp_f32 v62, v62\n\t"
    "v_mfma_f32_16x16x32_bf16 v[216:219], a[212:215], v[36:39], v[216:219]\n\t"
    "v_exp_f32 v63, v63\n\t"
    "v_mfma_f32_16x16x32_bf16 v[220:223], a[216:219], v[32:35], v[220:223]\n\t"
    "v_cvt_pk_bf16_f32 v46, v60, v61\n\t"
    "v_cvt_pk_bf16_f32 v47, v62, v63\n\t"
    "v_mfma_f32_16x16x32_bf16 v[220:223], a[220:223], v[36:39], v[220:223]\n\t"
    "s_waitcnt lgkmcnt(15)\n\t"
    "v_mfma_f32_16x16x32_bf16 v[48:51], a[96:99], a[0:3], 0\n\t"
    "s_waitcnt lgkmcnt(14)\n\t"
    "v_mfma_f32_16x16x32_bf16 v[48:51], a[100:103], a[4:7], v[48:51]\n\t"
    "s_waitcnt lgkmcnt(13)\n\t"
    "v_mfma_f32_16x16x32_bf16 v[48:51], a[104:107], a[8:11], v[48:51]\n\t"
    "s_waitcnt lgkmcnt(12)\n\t"
    "v_mfma_f32_16x16x32_bf16 v[48:51], a[108:111], a[12:15], v[48:51]\n\t"
    "s_waitcnt lgkmcnt(11)\n\t"
    "v_mfma_f32_16x16x32_bf16 v[52:55], a[112:115], a[0:3], 0\n\t"
    "s_waitcnt lgkmcnt(10)\n\t"
    "v_mfma_f32_16x16x32_bf16 v[52:55], a[116:119], a[4:7], v[52:55]\n\t"
    "s_waitcnt lgkmcnt(9)\n\t"
    "v_mfma_f32_16x16x32_bf16 v[52:55], a[120:123], a[8:11], v[52:55]\n\t"
    "s_waitcnt lgkmcnt(8)\n\t"
    "v_mfma_f32_16x16x32_bf16 v[52:55], a[124:127], a[12:15], v[52:55]\n\t"
    "s_waitcnt lgkmcnt(7)\n\t"
    "v_exp_f32 v48, v48\n\t"
    "v_mfma_f32_16x16x32_bf16 v[56:59], a[128:131], a[0:3], 0\n\t"
    "s_waitcnt lgkmcnt(6)\n\t"
    "v_exp_f32 v49, v49\n\t"
    "v_mfma_f32_16x16x32_bf16 v[56:59], a[132:135], a[4:7], v[56:59]\n\t"
    "s_waitcnt lgkmcnt(5)\n\t"
    "v_exp_f32 v50, v50\n\t"
    "v_mfma_f32_16x16x32_bf16 v[56:59], a[136:139], a[8:11], v[56:59]\n\t"
    "s_waitcnt lgkmcnt(4)\n\t"
    "v_exp_f32 v51, v51\n\t"
    "v_mfma_f32_16x16x32_bf16 v[56:59], a[140:143], a[12:15], v[56:59]\n\t"
    "s_waitcnt lgkmcnt(3)\n\t"
    "v_cvt_pk_bf16_f32 v32, v48, v49\n\t"
    "v_cvt_pk_bf16_f32 v33, v50, v51\n\t"
    "v_mfma_f32_16x16x32_bf16 v[60:63], a[144:147], a[0:3], 0\n\t"
    "s_waitcnt lgkmcnt(2)\n\t"
    "v_exp_f32 v52, v52\n\t"
    "v_mfma_f32_16x16x32_bf16 v[60:63], a[148:151], a[4:7], v[60:63]\n\t"
    "s_waitcnt lgkmcnt(1)\n\t"
    "v_exp_f32 v53, v53\n\t"
    "v_mfma_f32_16x16x32_bf16 v[60:63], a[152:155], a[8:11], v[60:63]\n\t"
    "s_waitcnt lgkmcnt(0)\n\t"
    "v_exp_f32 v54, v54\n\t"
    "v_mfma_f32_16x16x32_bf16 v[60:63], a[156:159], a[12:15], v[60:63]\n\t"
    "v_exp_f32 v55, v55\n\t"
    "v_smfmac_f32_16x16x64_bf16 a[244:247], a[248:251], v[40:47], v3\n\t"
    "v_cvt_pk_bf16_f32 v34, v52, v53\n\t"
    "v_cvt_pk_bf16_f32 v35, v54, v55\n\t"
    "v_mfma_f32_16x16x32_bf16 v[224:227], a[160:163], v[40:43], v[224:227]\n\t"
    "v_exp_f32 v56, v56\n\t"
    "v_mfma_f32_16x16x32_bf16 v[224:227], a[164:167], v[44:47], v[224:227]\n\t"
    "v_exp_f32 v57, v57\n\t"
    "v_mfma_f32_16x16x32_bf16 v[228:231], a[168:171], v[40:43], v[228:231]\n\t"
    "v_exp_f32 v58, v58\n\t"
    "v_mfma_f32_16x16x32_bf16 v[228:231], a[172:175], v[44:47], v[228:231]\n\t"
    "v_exp_f32 v59, v59\n\t"
    "v_mfma_f32_16x16x32_bf16 v[232:235], a[176:179], v[40:43], v[232:235]\n\t"
    "ds_read_b64_tr_b16 a[160:161], v1 offset:0\n\t"
    "v_cvt_pk_bf16_f32 v36, v56, v57\n\t"
    "v_mfma_f32_16x16x32_bf16 v[232:235], a[180:183], v[44:47], v[232:235]\n\t"
    "ds_read_b64_tr_b16 a[162:163], v1 offset:4096\n\t"
    "v_cvt_pk_bf16_f32 v37, v58, v59\n\t"
    "v_mfma_f32_16x16x32_bf16 v[236:239], a[184:187], v[40:43], v[236:239]\n\t"
    "v_exp_f32 v60, v60\n\t"
    "v_mfma_f32_16x16x32_bf16 v[236:239], a[188:191], v[44:47], v[236:239]\n\t"
    "v_exp_f32 v61, v61\n\t"
    "v_mfma_f32_16x16x32_bf16 v[240:243], a[192:195], v[40:43], v[240:243]\n\t"
    "v_exp_f32 v62, v62\n\t"
    "v_mfma_f32_16x16x32_bf16 v[240:243], a[196:199], v[44:47], v[240:243]\n\t"
    "v_exp_f32 v63, v63\n\t"
    "v_mfma_f32_16x16x32_bf16 v[244:247], a[200:203], v[40:43], v[244:247]\n\t"
    "ds_read_b64_tr_b16 a[164:165], v1 offset:8192\n\t"
    "v_cvt_pk_bf16_f32 v38, v60, v61\n\t"
    "v_mfma_f32_16x16x32_bf16 v[244:247], a[204:207], v[44:47], v[244:247]\n\t"
    "ds_read_b64_tr_b16 a[166:167], v1 offset:12288\n\t"
    "v_cvt_pk_bf16_f32 v39, v62, v63\n\t"
    "v_mfma_f32_16x16x32_bf16 v[248:251], a[208:211], v[40:43], v[248:251]\n\t"
    "ds_read_b64_tr_b16 a[168:169], v2 offset:0\n\t"
    "ds_read_b64_tr_b16 a[170:171], v2 offset:4096\n\t"
    "v_mfma_f32_16x16x32_bf16 v[248:251], a[212:215], v[44:47], v[248:251]\n\t"
    "ds_read_b64_tr_b16 a[172:173], v2 offset:8192\n\t"
    "ds_read_b64_tr_b16 a[174:175], v2 offset:12288\n\t"
    "v_mfma_f32_16x16x32_bf16 v[252:255], a[216:219], v[40:43], v[252:255]\n\t"
    "ds_read_b64_tr_b16 a[176:177], v1 offset:512\n\t"
    "ds_read_b64_tr_b16 a[178:179], v1 offset:4608\n\t"
    "v_mfma_f32_16x16x32_bf16 v[252:255], a[220:223], v[44:47], v[252:255]\n\t"
    "ds_read_b64_tr_b16 a[180:181], v1 offset:8704\n\t"
    "ds_read_b64_tr_b16 a[182:183], v1 offset:12800\n\t"
    "v_mfma_f32_16x16x32_bf16 v[48:51], a[96:99], a[16:19], 0\n\t"
    "ds_read_b64_tr_b16 a[184:185], v2 offset:512\n\t"
    "ds_read_b64_tr_b16 a[186:187], v2 offset:4608\n\t"
    "v_mfma_f32_16x16x32_bf16 v[48:51], a[100:103], a[20:23], v[48:51]\n\t"
    "ds_read_b64_tr_b16 a[188:189], v2 offset:8704\n\t"
    "ds_read_b64_tr_b16 a[190:191], v2 offset:12800\n\t"
    "v_mfma_f32_16x16x32_bf16 v[48:51], a[104:107], a[24:27], v[48:51]\n\t"
    "ds_read_b64_tr_b16 a[192:193], v1 offset:1024\n\t"
    "ds_read_b64_tr_b16 a[194:195], v1 offset:5120\n\t"
    "v_mfma_f32_16x16x32_bf16 v[48:51], a[108:111], a[28:31], v[48:51]\n\t"
    "ds_read_b64_tr_b16 a[196:197], v1 offset:9216\n\t"
    "ds_read_b64_tr_b16 a[198:199], v1 offset:13312\n\t"
    "v_mfma_f32_16x16x32_bf16 v[52:55], a[112:115], a[16:19], 0\n\t"
    "ds_read_b64_tr_b16 a[200:201], v2 offset:1024\n\t"
    "ds_read_b64_tr_b16 a[202:203], v2 offset:5120\n\t"
    "v_mfma_f32_16x16x32_bf16 v[52:55], a[116:119], a[20:23], v[52:55]\n\t"
    "ds_read_b64_tr_b16 a[204:205], v2 offset:9216\n\t"
    "ds_read_b64_tr_b16 a[206:207], v2 offset:13312\n\t"
    "v_mfma_f32_16x16x32_bf16 v[52:55], a[120:123], a[24:27], v[52:55]\n\t"
    "v_exp_f32 v48, v48\n\t"
    "v_mfma_f32_16x16x32_bf16 v[52:55], a[124:127], a[28:31], v[52:55]\n\t"
    "v_exp_f32 v49, v49\n\t"
    "v_mfma_f32_16x16x32_bf16 v[56:59], a[128:131], a[16:19], 0\n\t"
    "v_exp_f32 v50, v50\n\t"
    "v_mfma_f32_16x16x32_bf16 v[56:59], a[132:135], a[20:23], v[56:59]\n\t"
    "v_exp_f32 v51, v51\n\t"
    "v_mfma_f32_16x16x32_bf16 v[56:59], a[136:139], a[24:27], v[56:59]\n\t"
    "ds_read_b64_tr_b16 a[208:209], v1 offset:1536\n\t"
    "v_cvt_pk_bf16_f32 v40, v48, v49\n\t"
    "v_mfma_f32_16x16x32_bf16 v[56:59], a[140:143], a[28:31], v[56:59]\n\t"
    "ds_read_b64_tr_b16 a[210:211], v1 offset:5632\n\t"
    "v_cvt_pk_bf16_f32 v41, v50, v51\n\t"
    "v_mfma_f32_16x16x32_bf16 v[60:63], a[144:147], a[16:19], 0\n\t"
    "ds_read_b64_tr_b16 a[212:213], v1 offset:9728\n\t"
    "ds_read_b64_tr_b16 a[214:215], v1 offset:13824\n\t"
    "v_mfma_f32_16x16x32_bf16 v[60:63], a[148:151], a[20:23], v[60:63]\n\t"
    "v_exp_f32 v52, v52\n\t"
    "v_mfma_f32_16x16x32_bf16 v[60:63], a[152:155], a[24:27], v[60:63]\n\t"
    "ds_read_b64_tr_b16 a[216:217], v2 offset:1536\n\t"
    "ds_read_b64_tr_b16 a[218:219], v2 offset:5632\n\t"
    "v_mfma_f32_16x16x32_bf16 v[60:63], a[156:159], a[28:31], v[60:63]\n\t"
    "v_exp_f32 v53, v53\n\t"
    "v_smfmac_f32_16x16x64_bf16 a[224:227], a[248:251], v[32:39], v3\n\t"
    "s_waitcnt lgkmcnt(15)\n\t"
    "ds_read_b64_tr_b16 a[220:221], v2 offset:9728\n\t"
    "ds_read_b64_tr_b16 a[222:223], v2 offset:13824\n\t"
    "v_mfma_f32_16x16x32_bf16 v[64:67], a[160:163], v[32:35], v[64:67]\n\t"
    "v_exp_f32 v54, v54\n\t"
    "v_mfma_f32_16x16x32_bf16 v[64:67], a[164:167], v[36:39], v[64:67]\n\t"
    "v_exp_f32 v55, v55\n\t"
    "v_mfma_f32_16x16x32_bf16 v[68:71], a[168:171], v[32:35], v[68:71]\n\t"
    "v_cvt_pk_bf16_f32 v42, v52, v53\n\t"
    "v_cvt_pk_bf16_f32 v43, v54, v55\n\t"
    "v_mfma_f32_16x16x32_bf16 v[68:71], a[172:175], v[36:39], v[68:71]\n\t"
    "v_exp_f32 v56, v56\n\t"
    "v_mfma_f32_16x16x32_bf16 v[72:75], a[176:179], v[32:35], v[72:75]\n\t"
    "v_exp_f32 v57, v57\n\t"
    "v_mfma_f32_16x16x32_bf16 v[72:75], a[180:183], v[36:39], v[72:75]\n\t"
    "v_exp_f32 v58, v58\n\t"
    "v_mfma_f32_16x16x32_bf16 v[76:79], a[184:187], v[32:35], v[76:79]\n\t"
    "s_waitcnt lgkmcnt(15)\n\t"
    "v_exp_f32 v59, v59\n\t"
    "v_mfma_f32_16x16x32_bf16 v[76:79], a[188:191], v[36:39], v[76:79]\n\t"
    "s_waitcnt lgkmcnt(14)\n\t"
    "v_cvt_pk_bf16_f32 v44, v56, v57\n\t"
    "v_xor_b32 v1, 0x4000, v1\n\t"
    "v_mfma_f32_16x16x32_bf16 v[80:83], a[192:195], v[32:35], v[80:83]\n\t"
    "s_waitcnt lgkmcnt(12)\n\t"
    "v_cvt_pk_bf16_f32 v45, v58, v59\n\t"
    "v_xor_b32 v2, 0x4000, v2\n\t"
    "v_mfma_f32_16x16x32_bf16 v[80:83], a[196:199], v[36:39], v[80:83]\n\t"
    "s_waitcnt lgkmcnt(10)\n\t"
    "v_mfma_f32_16x16x32_bf16 v[84:87], a[200:203], v[32:35], v[84:87]\n\t"
    "s_waitcnt lgkmcnt(8)\n\t"
    "v_mfma_f32_16x16x32_bf16 v[84:87], a[204:207], v[36:39], v[84:87]\n\t"
    "s_waitcnt lgkmcnt(6)\n\t"
    "v_mfma_f32_16x16x32_bf16 v[88:91], a[208:211], v[32:35], v[88:91]\n\t"
    "s_waitcnt lgkmcnt(4)\n\t"
    "v_mfma_f32_16x16x32_bf16 v[88:91], a[212:215], v[36:39], v[88:91]\n\t"
    "s_waitcnt lgkmcnt(2)\n\t"
    "v_mfma_f32_16x16x32_bf16 v[92:95], a[216:219], v[32:35], v[92:95]\n\t"
    "s_waitcnt lgkmcnt(0)\n\t"
    "v_mfma_f32_16x16x32_bf16 v[92:95], a[220:223], v[36:39], v[92:95]\n\t"
    "s_cmp_lg_u32 s57, 0\n\t"
    "s_cbranch_scc1 L_L1\n\t"
    "v_mfma_f32_16x16x32_bf16 v[48:51], a[96:99], a[32:35], 0\n\t"
    "s_waitcnt lgkmcnt(0)\n\t"
    "v_subrev_u32 v8, 30464, v8\n\t"
    "v_accvgpr_read_b32 v14, a224\n\t"
    "v_mfma_f32_16x16x32_bf16 v[48:51], a[100:103], a[36:39], v[48:51]\n\t"
    "s_mov_b32 s50, s67\n\t"
    "v_accvgpr_read_b32 v15, a225\n\t"
    "v_add_f32 v11, v14, v15\n\t"
    "v_mfma_f32_16x16x32_bf16 v[48:51], a[104:107], a[40:43], v[48:51]\n\t"
    "s_mul_i32 s62, s56, 24576\n\t"
    "v_subrev_u32 v14, s54, v11\n\t"
    "v_max_u32 v10, v10, v14\n\t"
    "v_mfma_f32_16x16x32_bf16 v[48:51], a[108:111], a[44:47], v[48:51]\n\t"
    "s_add_u32 s62, s62, 98304\n\t"
    "v_rcp_f32 v11, v11\n\t"
    "v_mfma_f32_16x16x32_bf16 v[52:55], a[112:115], a[32:35], 0\n\t"
    "s_add_u32 s64, s62, 0\n\t"
    "v_mul_f32 v64, v11, v64\n\t"
    "v_mul_f32 v65, v11, v65\n\t"
    "v_mfma_f32_16x16x32_bf16 v[52:55], a[116:119], a[36:39], v[52:55]\n\t"
    "v_mul_f32 v66, v11, v66\n\t"
    "v_mul_f32 v67, v11, v67\n\t"
    "v_mfma_f32_16x16x32_bf16 v[52:55], a[120:123], a[40:43], v[52:55]\n\t"
    "v_cvt_pk_bf16_f32 v64, v64, v65\n\t"
    "v_cvt_pk_bf16_f32 v65, v66, v67\n\t"
    "ds_write_b64 v7, v[64:65] offset:0\n\t"
    "v_mfma_f32_16x16x32_bf16 v[52:55], a[124:127], a[44:47], v[52:55]\n\t"
    "v_exp_f32 v60, v60\n\t"
    "v_mfma_f32_16x16x32_bf16 v[56:59], a[128:131], a[32:35], 0\n\t"
    "v_exp_f32 v61, v61\n\t"
    "v_mfma_f32_16x16x32_bf16 v[56:59], a[132:135], a[36:39], v[56:59]\n\t"
    "v_mul_f32 v68, v11, v68\n\t"
    "v_mul_f32 v69, v11, v69\n\t"
    "v_mul_f32 v70, v11, v70\n\t"
    "v_mul_f32 v71, v11, v71\n\t"
    "v_cvt_pk_bf16_f32 v68, v68, v69\n\t"
    "v_cvt_pk_bf16_f32 v69, v70, v71\n\t"
    "ds_write_b64 v7, v[68:69] offset:32\n\t"
    "v_mfma_f32_16x16x32_bf16 v[56:59], a[136:139], a[40:43], v[56:59]\n\t"
    "v_exp_f32 v62, v62\n\t"
    "v_exp_f32 v63, v63\n\t"
    "v_mfma_f32_16x16x32_bf16 v[56:59], a[140:143], a[44:47], v[56:59]\n\t"
    "v_cvt_pk_bf16_f32 v46, v60, v61\n\t"
    "v_cvt_pk_bf16_f32 v47, v62, v63\n\t"
    "v_mfma_f32_16x16x32_bf16 v[60:63], a[144:147], a[32:35], 0\n\t"
    "v_mul_f32 v72, v11, v72\n\t"
    "v_mul_f32 v73, v11, v73\n\t"
    "v_mul_f32 v74, v11, v74\n\t"
    "v_mul_f32 v75, v11, v75\n\t"
    "v_cvt_pk_bf16_f32 v72, v72, v73\n\t"
    "v_cvt_pk_bf16_f32 v73, v74, v75\n\t"
    "ds_write_b64 v7, v[72:73] offset:64\n\t"
    "v_mfma_f32_16x16x32_bf16 v[60:63], a[148:151], a[36:39], v[60:63]\n\t"
    "v_mul_f32 v76, v11, v76\n\t"
    "v_mul_f32 v77, v11, v77\n\t"
    "v_mfma_f32_16x16x32_bf16 v[60:63], a[152:155], a[40:43], v[60:63]\n\t"
    "v_mul_f32 v78, v11, v78\n\t"
    "v_mul_f32 v79, v11, v79\n\t"
    "v_mfma_f32_16x16x32_bf16 v[60:63], a[156:159], a[44:47], v[60:63]\n\t"
    "v_cvt_pk_bf16_f32 v76, v76, v77\n\t"
    "v_cvt_pk_bf16_f32 v77, v78, v79\n\t"
    "ds_write_b64 v7, v[76:77] offset:96\n\t"
    "v_smfmac_f32_16x16x64_bf16 a[228:231], a[248:251], v[40:47], v3\n\t"
    "v_mul_f32 v80, v11, v80\n\t"
    "v_mul_f32 v81, v11, v81\n\t"
    "v_mfma_f32_16x16x32_bf16 v[96:99], a[160:163], v[40:43], v[96:99]\n\t"
    "v_mul_f32 v82, v11, v82\n\t"
    "v_mul_f32 v83, v11, v83\n\t"
    "v_mfma_f32_16x16x32_bf16 v[96:99], a[164:167], v[44:47], v[96:99]\n\t"
    "v_cvt_pk_bf16_f32 v80, v80, v81\n\t"
    "v_cvt_pk_bf16_f32 v81, v82, v83\n\t"
    "ds_write_b64 v7, v[80:81] offset:128\n\t"
    "v_mfma_f32_16x16x32_bf16 v[100:103], a[168:171], v[40:43], v[100:103]\n\t"
    "v_mul_f32 v84, v11, v84\n\t"
    "v_mul_f32 v85, v11, v85\n\t"
    "v_mfma_f32_16x16x32_bf16 v[100:103], a[172:175], v[44:47], v[100:103]\n\t"
    "v_mul_f32 v86, v11, v86\n\t"
    "v_mul_f32 v87, v11, v87\n\t"
    "v_mfma_f32_16x16x32_bf16 v[104:107], a[176:179], v[40:43], v[104:107]\n\t"
    "v_cvt_pk_bf16_f32 v84, v84, v85\n\t"
    "v_cvt_pk_bf16_f32 v85, v86, v87\n\t"
    "ds_write_b64 v7, v[84:85] offset:160\n\t"
    "v_mfma_f32_16x16x32_bf16 v[104:107], a[180:183], v[44:47], v[104:107]\n\t"
    "v_mul_f32 v88, v11, v88\n\t"
    "v_mul_f32 v89, v11, v89\n\t"
    "v_mfma_f32_16x16x32_bf16 v[108:111], a[184:187], v[40:43], v[108:111]\n\t"
    "v_mul_f32 v90, v11, v90\n\t"
    "v_mul_f32 v91, v11, v91\n\t"
    "v_mfma_f32_16x16x32_bf16 v[108:111], a[188:191], v[44:47], v[108:111]\n\t"
    "v_cvt_pk_bf16_f32 v88, v88, v89\n\t"
    "v_cvt_pk_bf16_f32 v89, v90, v91\n\t"
    "ds_write_b64 v7, v[88:89] offset:192\n\t"
    "v_mfma_f32_16x16x32_bf16 v[112:115], a[192:195], v[40:43], v[112:115]\n\t"
    "v_exp_f32 v48, v48\n\t"
    "v_mfma_f32_16x16x32_bf16 v[112:115], a[196:199], v[44:47], v[112:115]\n\t"
    "v_accvgpr_read_b32 v14, a228\n\t"
    "v_accvgpr_read_b32 v15, a229\n\t"
    "v_add_f32 v12, v14, v15\n\t"
    "v_subrev_u32 v14, s54, v12\n\t"
    "v_max_u32 v10, v10, v14\n\t"
    "v_rcp_f32 v12, v12\n\t"
    "s_nop 0\n\t"
    "v_mul_f32 v96, v12, v96\n\t"
    "v_mul_f32 v97, v12, v97\n\t"
    "v_mul_f32 v98, v12, v98\n\t"
    "v_mul_f32 v99, v12, v99\n\t"
    "v_cvt_pk_bf16_f32 v96, v96, v97\n\t"
    "v_cvt_pk_bf16_f32 v97, v98, v99\n\t"
    "ds_write_b64 v7, v[96:97] offset:4352\n\t"
    "v_mfma_f32_16x16x32_bf16 v[116:119], a[200:203], v[40:43], v[116:119]\n\t"
    "v_mul_f32 v92, v11, v92\n\t"
    "v_mul_f32 v93, v11, v93\n\t"
    "v_mul_f32 v94, v11, v94\n\t"
    "v_mul_f32 v95, v11, v95\n\t"
    "v_cvt_pk_bf16_f32 v92, v92, v93\n\t"
    "v_cvt_pk_bf16_f32 v93, v94, v95\n\t"
    "ds_write_b64 v7, v[92:93] offset:224\n\t"
    "v_mfma_f32_16x16x32_bf16 v[116:119], a[204:207], v[44:47], v[116:119]\n\t"
    "v_exp_f32 v49, v49\n\t"
    "v_mul_f32 v100, v12, v100\n\t"
    "v_mul_f32 v101, v12, v101\n\t"
    "v_mul_f32 v102, v12, v102\n\t"
    "v_mul_f32 v103, v12, v103\n\t"
    "v_cvt_pk_bf16_f32 v100, v100, v101\n\t"
    "v_cvt_pk_bf16_f32 v101, v102, v103\n\t"
    "ds_write_b64 v7, v[100:101] offset:4384\n\t"
    "v_mfma_f32_16x16x32_bf16 v[120:123], a[208:211], v[40:43], v[120:123]\n\t"
    "v_exp_f32 v50, v50\n\t"
    "v_mfma_f32_16x16x32_bf16 v[120:123], a[212:215], v[44:47], v[120:123]\n\t"
    "v_exp_f32 v51, v51\n\t"
    "v_mul_f32 v104, v12, v104\n\t"
    "v_mul_f32 v105, v12, v105\n\t"
    "v_mul_f32 v106, v12, v106\n\t"
    "v_mul_f32 v107, v12, v107\n\t"
    "v_cvt_pk_bf16_f32 v104, v104, v105\n\t"
    "v_cvt_pk_bf16_f32 v105, v106, v107\n\t"
    "ds_write_b64 v7, v[104:105] offset:4416\n\t"
    "v_mfma_f32_16x16x32_bf16 v[124:127], a[216:219], v[40:43], v[124:127]\n\t"
    "ds_read_b128 v[16:19], v8 offset:0\n\t"
    "v_cvt_pk_bf16_f32 v32, v48, v49\n\t"
    "v_mfma_f32_16x16x32_bf16 v[124:127], a[220:223], v[44:47], v[124:127]\n\t"
    "ds_read_b128 v[20:23], v8 offset:1088\n\t"
    "v_cvt_pk_bf16_f32 v33, v50, v51\n\t"
    "v_mul_f32 v108, v12, v108\n\t"
    "v_mul_f32 v109, v12, v109\n\t"
    "v_mul_f32 v110, v12, v110\n\t"
    "v_mul_f32 v111, v12, v111\n\t"
    "v_cvt_pk_bf16_f32 v108, v108, v109\n\t"
    "v_cvt_pk_bf16_f32 v109, v110, v111\n\t"
    "ds_write_b64 v7, v[108:109] offset:4448\n\t"
    "v_mfma_f32_16x16x32_bf16 v[48:51], a[96:99], a[48:51], 0\n\t"
    "v_exp_f32 v52, v52\n\t"
    "v_mfma_f32_16x16x32_bf16 v[48:51], a[100:103], a[52:55], v[48:51]\n\t"
    "v_exp_f32 v53, v53\n\t"
    "v_mul_f32 v112, v12, v112\n\t"
    "v_mul_f32 v113, v12, v113\n\t"
    "v_mul_f32 v114, v12, v114\n\t"
    "v_mul_f32 v115, v12, v115\n\t"
    "v_cvt_pk_bf16_f32 v112, v112, v113\n\t"
    "v_cvt_pk_bf16_f32 v113, v114, v115\n\t"
    "ds_write_b64 v7, v[112:113] offset:4480\n\t"
    "v_mfma_f32_16x16x32_bf16 v[48:51], a[104:107], a[56:59], v[48:51]\n\t"
    "v_exp_f32 v54, v54\n\t"
    "s_waitcnt lgkmcnt(3)\n\t"
    "buffer_store_dwordx4 v[16:19], v9, s[48:51], s64 offen offset:0 sc1\n\t"
    "v_exp_f32 v55, v55\n\t"
    "v_mfma_f32_16x16x32_bf16 v[48:51], a[108:111], a[60:63], v[48:51]\n\t"
    "ds_read_b128 v[16:19], v8 offset:2176\n\t"
    "v_cvt_pk_bf16_f32 v34, v52, v53\n\t"
    "v_cvt_pk_bf16_f32 v35, v54, v55\n\t"
    "v_mul_f32 v116, v12, v116\n\t"
    "v_mul_f32 v117, v12, v117\n\t"
    "v_mul_f32 v118, v12, v118\n\t"
    "v_mul_f32 v119, v12, v119\n\t"
    "v_cvt_pk_bf16_f32 v116, v116, v117\n\t"
    "v_cvt_pk_bf16_f32 v117, v118, v119\n\t"
    "ds_write_b64 v7, v[116:117] offset:4512\n\t"
    "v_mfma_f32_16x16x32_bf16 v[52:55], a[112:115], a[48:51], 0\n\t"
    "v_exp_f32 v56, v56\n\t"
    "v_mfma_f32_16x16x32_bf16 v[52:55], a[116:119], a[52:55], v[52:55]\n\t"
    "v_exp_f32 v57, v57\n\t"
    "v_mul_f32 v120, v12, v120\n\t"
    "v_mul_f32 v121, v12, v121\n\t"
    "v_mul_f32 v122, v12, v122\n\t"
    "v_mul_f32 v123, v12, v123\n\t"
    "v_cvt_pk_bf16_f32 v120, v120, v121\n\t"
    "v_cvt_pk_bf16_f32 v121, v122, v123\n\t"
    "ds_write_b64 v7, v[120:121] offset:4544\n\t"
    "v_mfma_f32_16x16x32_bf16 v[52:55], a[120:123], a[56:59], v[52:55]\n\t"
    "s_waitcnt lgkmcnt(5)\n\t"
    "buffer_store_dwordx4 v[20:23], v9, s[48:51], s64 offen offset:1024 sc1\n\t"
    "v_exp_f32 v58, v58\n\t"
    "v_exp_f32 v59, v59\n\t"
    "v_mfma_f32_16x16x32_bf16 v[52:55], a[124:127], a[60:63], v[52:55]\n\t"
    "ds_read_b128 v[20:23], v8 offset:3264\n\t"
    "v_mul_f32 v124, v12, v124\n\t"
    "v_mul_f32 v125, v12, v125\n\t"
    "v_mul_f32 v126, v12, v126\n\t"
    "v_mul_f32 v127, v12, v127\n\t"
    "v_cvt_pk_bf16_f32 v124, v124, v125\n\t"
    "v_cvt_pk_bf16_f32 v125, v126, v127\n\t"
    "ds_write_b64 v7, v[124:125] offset:4576\n\t"
    "v_cvt_pk_bf16_f32 v36, v56, v57\n\t"
    "v_cvt_pk_bf16_f32 v37, v58, v59\n\t"
    "v_mfma_f32_16x16x32_bf16 v[56:59], a[128:131], a[48:51], 0\n\t"
    "v_exp_f32 v60, v60\n\t"
    "v_mfma_f32_16x16x32_bf16 v[56:59], a[132:135], a[52:55], v[56:59]\n\t"
    "v_exp_f32 v61, v61\n\t"
    "v_mfma_f32_16x16x32_bf16 v[56:59], a[136:139], a[56:59], v[56:59]\n\t"
    "s_waitcnt lgkmcnt(4)\n\t"
    "buffer_store_dwordx4 v[16:19], v9, s[48:51], s64 offen offset:2048 sc1\n\t"
    "v_exp_f32 v62, v62\n\t"
    "v_exp_f32 v63, v63\n\t"
    "v_mfma_f32_16x16x32_bf16 v[56:59], a[140:143], a[60:63], v[56:59]\n\t"
    "v_cvt_pk_bf16_f32 v38, v60, v61\n\t"
    "v_cvt_pk_bf16_f32 v39, v62, v63\n\t"
    "v_mfma_f32_16x16x32_bf16 v[60:63], a[144:147], a[48:51], 0\n\t"
    "v_exp_f32 v48, v48\n\t"
    "v_mfma_f32_16x16x32_bf16 v[60:63], a[148:151], a[52:55], v[60:63]\n\t"
    "s_waitcnt lgkmcnt(1)\n\t"
    "buffer_store_dwordx4 v[20:23], v9, s[48:51], s64 offen offset:3072 sc1\n\t"
    "ds_read_b128 v[16:19], v8 offset:4352\n\t"
    "v_mfma_f32_16x16x32_bf16 v[60:63], a[152:155], a[56:59], v[60:63]\n\t"
    "s_add_u32 s63, s62, 4096\n\t"
    "ds_read_b128 v[20:23], v8 offset:5440\n\t"
    "v_mfma_f32_16x16x32_bf16 v[60:63], a[156:159], a[60:63], v[60:63]\n\t"
    "s_waitcnt lgkmcnt(1)\n\t"
    "buffer_store_dwordx4 v[16:19], v9, s[48:51], s63 offen offset:0 sc1\n\t"
    "v_smfmac_f32_16x16x64_bf16 a[232:235], a[248:251], v[32:39], v3\n\t"
    "v_exp_f32 v49, v49\n\t"
    "v_mfma_f32_16x16x32_bf16 v[128:131], a[160:163], v[32:35], v[128:131]\n\t"
    "ds_read_b128 v[16:19], v8 offset:6528\n\t"
    "v_mfma_f32_16x16x32_bf16 v[128:131], a[164:167], v[36:39], v[128:131]\n\t"
    "v_exp_f32 v50, v50\n\t"
    "v_mfma_f32_16x16x32_bf16 v[132:135], a[168:171], v[32:35], v[132:135]\n\t"
    "s_waitcnt lgkmcnt(1)\n\t"
    "buffer_store_dwordx4 v[20:23], v9, s[48:51], s63 offen offset:1024 sc1\n\t"
    "v_accvgpr_read_b32 v14, a232\n\t"
    "v_mfma_f32_16x16x32_bf16 v[132:135], a[172:175], v[36:39], v[132:135]\n\t"
    "ds_read_b128 v[20:23], v8 offset:7616\n\t"
    "v_accvgpr_read_b32 v15, a233\n\t"
    "v_mfma_f32_16x16x32_bf16 v[136:139], a[176:179], v[32:35], v[136:139]\n\t"
    "v_add_f32 v13, v14, v15\n\t"
    "v_subrev_u32 v14, s54, v13\n\t"
    "v_mfma_f32_16x16x32_bf16 v[136:139], a[180:183], v[36:39], v[136:139]\n\t"
    "v_max_u32 v10, v10, v14\n\t"
    "v_mfma_f32_16x16x32_bf16 v[140:143], a[184:187], v[32:35], v[140:143]\n\t"
    "s_waitcnt lgkmcnt(1)\n\t"
    "buffer_store_dwordx4 v[16:19], v9, s[48:51], s63 offen offset:2048 sc1\n\t"
    "v_mfma_f32_16x16x32_bf16 v[140:143], a[188:191], v[36:39], v[140:143]\n\t"
    "v_rcp_f32 v13, v13\n\t"
    "v_mfma_f32_16x16x32_bf16 v[144:147], a[192:195], v[32:35], v[144:147]\n\t"
    "v_mul_f32 v128, v13, v128\n\t"
    "v_mul_f32 v129, v13, v129\n\t"
    "v_mfma_f32_16x16x32_bf16 v[144:147], a[196:199], v[36:39], v[144:147]\n\t"
    "s_waitcnt lgkmcnt(0)\n\t"
    "buffer_store_dwordx4 v[20:23], v9, s[48:51], s63 offen offset:3072 sc1\n\t"
    "v_mul_f32 v130, v13, v130\n\t"
    "v_mul_f32 v131, v13, v131\n\t"
    "v_cvt_pk_bf16_f32 v128, v128, v129\n\t"
    "v_cvt_pk_bf16_f32 v129, v130, v131\n\t"
    "ds_write_b64 v7, v[128:129] offset:8704\n\t"
    "v_mfma_f32_16x16x32_bf16 v[148:151], a[200:203], v[32:35], v[148:151]\n\t"
    "s_add_u32 s64, s62, 8192\n\t"
    "v_mul_f32 v132, v13, v132\n\t"
    "v_mul_f32 v133, v13, v133\n\t"
    "v_mfma_f32_16x16x32_bf16 v[148:151], a[204:207], v[36:39], v[148:151]\n\t"
    "v_mul_f32 v134, v13, v134\n\t"
    "v_mul_f32 v135, v13, v135\n\t"
    "v_cvt_pk_bf16_f32 v132, v132, v133\n\t"
    "v_cvt_pk_bf16_f32 v133, v134, v135\n\t"
    "ds_write_b64 v7, v[132:133] offset:8736\n\t"
    "v_mfma_f32_16x16x32_bf16 v[152:155], a[208:211], v[32:35], v[152:155]\n\t"
    "v_exp_f32 v51, v51\n\t"
    "v_mfma_f32_16x16x32_bf16 v[152:155], a[212:215], v[36:39], v[152:155]\n\t"
    "v_mul_f32 v136, v13, v136\n\t"
    "v_mul_f32 v137, v13, v137\n\t"
    "v_mul_f32 v138, v13, v138\n\t"
    "v_mul_f32 v139, v13, v139\n\t"
    "v_cvt_pk_bf16_f32 v136, v136, v137\n\t"
    "v_cvt_pk_bf16_f32 v137, v138, v139\n\t"
    "ds_write_b64 v7, v[136:137] offset:8768\n\t"
    "v_mfma_f32_16x16x32_bf16 v[156:159], a[216:219], v[32:35], v[156:159]\n\t"
    "v_cvt_pk_bf16_f32 v40, v48, v49\n\t"
    "v_cvt_pk_bf16_f32 v41, v50, v51\n\t"
    "v_mfma_f32_16x16x32_bf16 v[156:159], a[220:223], v[36:39], v[156:159]\n\t"
    "v_exp_f32 v52, v52\n\t"
    "v_mul_f32 v140, v13, v140\n\t"
    "v_mul_f32 v141, v13, v141\n\t"
    "v_mul_f32 v142, v13, v142\n\t"
    "v_mul_f32 v143, v13, v143\n\t"
    "v_cvt_pk_bf16_f32 v140, v140, v141\n\t"
    "v_cvt_pk_bf16_f32 v141, v142, v143\n\t"
    "ds_write_b64 v7, v[140:141] offset:8800\n\t"
    "v_mfma_f32_16x16x32_bf16 v[48:51], a[96:99], a[64:67], 0\n\t"
    "v_exp_f32 v53, v53\n\t"
    "v_mfma_f32_16x16x32_bf16 v[48:51], a[100:103], a[68:71], v[48:51]\n\t"
    "v_exp_f32 v54, v54\n\t"
    "v_mul_f32 v144, v13, v144\n\t"
    "v_mul_f32 v145, v13, v145\n\t"
    "v_mul_f32 v146, v13, v146\n\t"
    "v_mul_f32 v147, v13, v147\n\t"
    "v_cvt_pk_bf16_f32 v144, v144, v145\n\t"
    "v_cvt_pk_bf16_f32 v145, v146, v147\n\t"
    "ds_write_b64 v7, v[144:145] offset:8832\n\t"
    "v_mfma_f32_16x16x32_bf16 v[48:51], a[104:107], a[72:75], v[48:51]\n\t"
    "v_exp_f32 v55, v55\n\t"
    "v_mfma_f32_16x16x32_bf16 v[48:51], a[108:111], a[76:79], v[48:51]\n\t"
    "v_cvt_pk_bf16_f32 v42, v52, v53\n\t"
    "v_cvt_pk_bf16_f32 v43, v54, v55\n\t"
    "v_mul_f32 v148, v13, v148\n\t"
    "v_mul_f32 v149, v13, v149\n\t"
    "v_mul_f32 v150, v13, v150\n\t"
    "v_mul_f32 v151, v13, v151\n\t"
    "v_cvt_pk_bf16_f32 v148, v148, v149\n\t"
    "v_cvt_pk_bf16_f32 v149, v150, v151\n\t"
    "ds_write_b64 v7, v[148:149] offset:8864\n\t"
    "v_mfma_f32_16x16x32_bf16 v[52:55], a[112:115], a[64:67], 0\n\t"
    "v_exp_f32 v56, v56\n\t"
    "v_mfma_f32_16x16x32_bf16 v[52:55], a[116:119], a[68:71], v[52:55]\n\t"
    "v_exp_f32 v57, v57\n\t"
    "v_mul_f32 v152, v13, v152\n\t"
    "v_mul_f32 v153, v13, v153\n\t"
    "v_mul_f32 v154, v13, v154\n\t"
    "v_mul_f32 v155, v13, v155\n\t"
    "v_cvt_pk_bf16_f32 v152, v152, v153\n\t"
    "v_cvt_pk_bf16_f32 v153, v154, v155\n\t"
    "ds_write_b64 v7, v[152:153] offset:8896\n\t"
    "v_mfma_f32_16x16x32_bf16 v[52:55], a[120:123], a[72:75], v[52:55]\n\t"
    "v_exp_f32 v58, v58\n\t"
    "v_exp_f32 v59, v59\n\t"
    "v_mfma_f32_16x16x32_bf16 v[52:55], a[124:127], a[76:79], v[52:55]\n\t"
    "v_mul_f32 v156, v13, v156\n\t"
    "v_mul_f32 v157, v13, v157\n\t"
    "v_mul_f32 v158, v13, v158\n\t"
    "v_mul_f32 v159, v13, v159\n\t"
    "v_cvt_pk_bf16_f32 v156, v156, v157\n\t"
    "v_cvt_pk_bf16_f32 v157, v158, v159\n\t"
    "ds_write_b64 v7, v[156:157] offset:8928\n\t"
    "v_cvt_pk_bf16_f32 v44, v56, v57\n\t"
    "v_cvt_pk_bf16_f32 v45, v58, v59\n\t"
    "v_mfma_f32_16x16x32_bf16 v[56:59], a[128:131], a[64:67], 0\n\t"
    "v_exp_f32 v60, v60\n\t"
    "v_mfma_f32_16x16x32_bf16 v[56:59], a[132:135], a[68:71], v[56:59]\n\t"
    "v_exp_f32 v61, v61\n\t"
    "v_mfma_f32_16x16x32_bf16 v[56:59], a[136:139], a[72:75], v[56:59]\n\t"
    "v_exp_f32 v62, v62\n\t"
    "v_exp_f32 v63, v63\n\t"
    "v_mfma_f32_16x16x32_bf16 v[56:59], a[140:143], a[76:79], v[56:59]\n\t"
    "ds_read_b128 v[16:19], v8 offset:8704\n\t"
    "v_cvt_pk_bf16_f32 v46, v60, v61\n\t"
    "v_cvt_pk_bf16_f32 v47, v62, v63\n\t"
    "v_mfma_f32_16x16x32_bf16 v[60:63], a[144:147], a[64:67], 0\n\t"
    "ds_read_b128 v[20:23], v8 offset:9792\n\t"
    "v_mfma_f32_16x16x32_bf16 v[60:63], a[148:151], a[68:71], v[60:63]\n\t"
    "v_exp_f32 v48, v48\n\t"
    "v_mfma_f32_16x16x32_bf16 v[60:63], a[152:155], a[72:75], v[60:63]\n\t"
    "v_exp_f32 v49, v49\n\t"
    "v_mfma_f32_16x16x32_bf16 v[60:63], a[156:159], a[76:79], v[60:63]\n\t"
    "s_waitcnt lgkmcnt(1)\n\t"
    "buffer_store_dwordx4 v[16:19], v9, s[48:51], s64 offen offset:0 sc1\n\t"
    "v_smfmac_f32_16x16x64_bf16 a[236:239], a[248:251], v[40:47], v3\n\t"
    "v_exp_f32 v50, v50\n\t"
    "v_mfma_f32_16x16x32_bf16 v[160:163], a[160:163], v[40:43], v[160:163]\n\t"
    "ds_read_b128 v[16:19], v8 offset:10880\n\t"
    "v_mfma_f32_16x16x32_bf16 v[160:163], a[164:167], v[44:47], v[160:163]\n\t"
    "s_waitcnt lgkmcnt(1)\n\t"
    "buffer_store_dwordx4 v[20:23], v9, s[48:51], s64 offen offset:1024 sc1\n\t"
    "v_mfma_f32_16x16x32_bf16 v[164:167], a[168:171], v[40:43], v[164:167]\n\t"
    "v_exp_f32 v51, v51\n\t"
    "v_mfma_f32_16x16x32_bf16 v[164:167], a[172:175], v[44:47], v[164:167]\n\t"
    "ds_read_b128 v[20:23], v8 offset:11968\n\t"
    "v_accvgpr_read_b32 v14, a236\n\t"
    "v_mfma_f32_16x16x32_bf16 v[168:171], a[176:179], v[40:43], v[168:171]\n\t"
    "v_accvgpr_read_b32 v15, a237\n\t"
    "v_add_f32 v11, v14, v15\n\t"
    "v_mfma_f32_16x16x32_bf16 v[168:171], a[180:183], v[44:47], v[168:171]\n\t"
    "v_subrev_u32 v14, s54, v11\n\t"
    "v_max_u32 v10, v10, v14\n\t"
    "v_mfma_f32_16x16x32_bf16 v[172:175], a[184:187], v[40:43], v[172:175]\n\t"
    "s_waitcnt lgkmcnt(1)\n\t"
    "buffer_store_dwordx4 v[16:19], v9, s[48:51], s64 offen offset:2048 sc1\n\t"
    "v_cvt_pk_bf16_f32 v32, v48, v49\n\t"
    "v_mfma_f32_16x16x32_bf16 v[172:175], a[188:191], v[44:47], v[172:175]\n\t"
    "v_rcp_f32 v11, v11\n\t"
    "v_mfma_f32_16x16x32_bf16 v[176:179], a[192:195], v[40:43], v[176:179]\n\t"
    "v_mul_f32 v160, v11, v160\n\t"
    "v_mul_f32 v161, v11, v161\n\t"
    "v_mfma_f32_16x16x32_bf16 v[176:179], a[196:199], v[44:47], v[176:179]\n\t"
    "s_waitcnt lgkmcnt(0)\n\t"
    "buffer_store_dwordx4 v[20:23], v9, s[48:51], s64 offen offset:3072 sc1\n\t"
    "v_mul_f32 v162, v11, v162\n\t"
    "v_mul_f32 v163, v11, v163\n\t"
    "v_cvt_pk_bf16_f32 v160, v160, v161\n\t"
    "v_cvt_pk_bf16_f32 v161, v162, v163\n\t"
    "ds_write_b64 v7, v[160:161] offset:13056\n\t"
    "v_mfma_f32_16x16x32_bf16 v[180:183], a[200:203], v[40:43], v[180:183]\n\t"
    "s_add_u32 s63, s62, 12288\n\t"
    "v_mul_f32 v164, v11, v164\n\t"
    "v_mul_f32 v165, v11, v165\n\t"
    "v_mfma_f32_16x16x32_bf16 v[180:183], a[204:207], v[44:47], v[180:183]\n\t"
    "v_mul_f32 v166, v11, v166\n\t"
    "v_mul_f32 v167, v11, v167\n\t"
    "v_cvt_pk_bf16_f32 v164, v164, v165\n\t"
    "v_cvt_pk_bf16_f32 v165, v166, v167\n\t"
    "ds_write_b64 v7, v[164:165] offset:13088\n\t"
    "v_mfma_f32_16x16x32_bf16 v[184:187], a[208:211], v[40:43], v[184:187]\n\t"
    "v_cvt_pk_bf16_f32 v33, v50, v51\n\t"
    "v_mul_f32 v168, v11, v168\n\t"
    "v_mfma_f32_16x16x32_bf16 v[184:187], a[212:215], v[44:47], v[184:187]\n\t"
    "v_mul_f32 v169, v11, v169\n\t"
    "v_mul_f32 v170, v11, v170\n\t"
    "v_mul_f32 v171, v11, v171\n\t"
    "v_cvt_pk_bf16_f32 v168, v168, v169\n\t"
    "v_cvt_pk_bf16_f32 v169, v170, v171\n\t"
    "ds_write_b64 v7, v[168:169] offset:13120\n\t"
    "v_mfma_f32_16x16x32_bf16 v[188:191], a[216:219], v[40:43], v[188:191]\n\t"
    "v_exp_f32 v52, v52\n\t"
    "v_mfma_f32_16x16x32_bf16 v[188:191], a[220:223], v[44:47], v[188:191]\n\t"
    "v_mul_f32 v172, v11, v172\n\t"
    "v_mul_f32 v173, v11, v173\n\t"
    "v_mul_f32 v174, v11, v174\n\t"
    "v_mul_f32 v175, v11, v175\n\t"
    "v_cvt_pk_bf16_f32 v172, v172, v173\n\t"
    "v_cvt_pk_bf16_f32 v173, v174, v175\n\t"
    "ds_write_b64 v7, v[172:173] offset:13152\n\t"
    "v_mfma_f32_16x16x32_bf16 v[48:51], a[96:99], a[80:83], 0\n\t"
    "v_exp_f32 v53, v53\n\t"
    "v_mfma_f32_16x16x32_bf16 v[48:51], a[100:103], a[84:87], v[48:51]\n\t"
    "v_exp_f32 v54, v54\n\t"
    "v_mul_f32 v176, v11, v176\n\t"
    "v_mul_f32 v177, v11, v177\n\t"
    "v_mul_f32 v178, v11, v178\n\t"
    "v_mul_f32 v179, v11, v179\n\t"
    "v_cvt_pk_bf16_f32 v176, v176, v177\n\t"
    "v_cvt_pk_bf16_f32 v177, v178, v179\n\t"
    "ds_write_b64 v7, v[176:177] offset:13184\n\t"
    "v_mfma_f32_16x16x32_bf16 v[48:51], a[104:107], a[88:91], v[48:51]\n\t"
    "v_exp_f32 v55, v55\n\t"
    "v_mfma_f32_16x16x32_bf16 v[48:51], a[108:111], a[92:95], v[48:51]\n\t"
    "v_cvt_pk_bf16_f32 v34, v52, v53\n\t"
    "v_cvt_pk_bf16_f32 v35, v54, v55\n\t"
    "v_mul_f32 v180, v11, v180\n\t"
    "v_mul_f32 v181, v11, v181\n\t"
    "v_mul_f32 v182, v11, v182\n\t"
    "v_mul_f32 v183, v11, v183\n\t"
    "v_cvt_pk_bf16_f32 v180, v180, v181\n\t"
    "v_cvt_pk_bf16_f32 v181, v182, v183\n\t"
    "ds_write_b64 v7, v[180:181] offset:13216\n\t"
    "v_mfma_f32_16x16x32_bf16 v[52:55], a[112:115], a[80:83], 0\n\t"
    "v_exp_f32 v56, v56\n\t"
    "v_mfma_f32_16x16x32_bf16 v[52:55], a[116:119], a[84:87], v[52:55]\n\t"
    "v_exp_f32 v57, v57\n\t"
    "v_mul_f32 v184, v11, v184\n\t"
    "v_mul_f32 v185, v11, v185\n\t"
    "v_mul_f32 v186, v11, v186\n\t"
    "v_mul_f32 v187, v11, v187\n\t"
    "v_cvt_pk_bf16_f32 v184, v184, v185\n\t"
    "v_cvt_pk_bf16_f32 v185, v186, v187\n\t"
    "ds_write_b64 v7, v[184:185] offset:13248\n\t"
    "v_mfma_f32_16x16x32_bf16 v[52:55], a[120:123], a[88:91], v[52:55]\n\t"
    "v_exp_f32 v58, v58\n\t"
    "v_exp_f32 v59, v59\n\t"
    "v_mfma_f32_16x16x32_bf16 v[52:55], a[124:127], a[92:95], v[52:55]\n\t"
    "v_mul_f32 v188, v11, v188\n\t"
    "v_mul_f32 v189, v11, v189\n\t"
    "v_mul_f32 v190, v11, v190\n\t"
    "v_mul_f32 v191, v11, v191\n\t"
    "v_cvt_pk_bf16_f32 v188, v188, v189\n\t"
    "v_cvt_pk_bf16_f32 v189, v190, v191\n\t"
    "ds_write_b64 v7, v[188:189] offset:13280\n\t"
    "v_cvt_pk_bf16_f32 v36, v56, v57\n\t"
    "v_cvt_pk_bf16_f32 v37, v58, v59\n\t"
    "v_mfma_f32_16x16x32_bf16 v[56:59], a[128:131], a[80:83], 0\n\t"
    "v_exp_f32 v60, v60\n\t"
    "v_mfma_f32_16x16x32_bf16 v[56:59], a[132:135], a[84:87], v[56:59]\n\t"
    "v_exp_f32 v61, v61\n\t"
    "v_mfma_f32_16x16x32_bf16 v[56:59], a[136:139], a[88:91], v[56:59]\n\t"
    "v_exp_f32 v62, v62\n\t"
    "v_exp_f32 v63, v63\n\t"
    "v_mfma_f32_16x16x32_bf16 v[56:59], a[140:143], a[92:95], v[56:59]\n\t"
    "ds_read_b128 v[16:19], v8 offset:13056\n\t"
    "v_cvt_pk_bf16_f32 v38, v60, v61\n\t"
    "v_cvt_pk_bf16_f32 v39, v62, v63\n\t"
    "v_mfma_f32_16x16x32_bf16 v[60:63], a[144:147], a[80:83], 0\n\t"
    "v_exp_f32 v48, v48\n\t"
    "v_mfma_f32_16x16x32_bf16 v[60:63], a[148:151], a[84:87], v[60:63]\n\t"
    "v_exp_f32 v49, v49\n\t"
    "v_mfma_f32_16x16x32_bf16 v[60:63], a[152:155], a[88:91], v[60:63]\n\t"
    "v_exp_f32 v50, v50\n\t"
    "v_mfma_f32_16x16x32_bf16 v[60:63], a[156:159], a[92:95], v[60:63]\n\t"
    "ds_read_b128 v[20:23], v8 offset:14144\n\t"
    "s_waitcnt lgkmcnt(1)\n\t"
    "buffer_store_dwordx4 v[16:19], v9, s[48:51], s63 offen offset:0 sc1\n\t"
    "v_smfmac_f32_16x16x64_bf16 a[240:243], a[248:251], v[32:39], v3\n\t"
    "v_exp_f32 v51, v51\n\t"
    "v_mfma_f32_16x16x32_bf16 v[192:195], a[160:163], v[32:35], v[192:195]\n\t"
    "ds_read_b128 v[16:19], v8 offset:15232\n\t"
    "v_cvt_pk_bf16_f32 v40, v48, v49\n\t"
    "v_mfma_f32_16x16x32_bf16 v[192:195], a[164:167], v[36:39], v[192:195]\n\t"
    "v_cvt_pk_bf16_f32 v41, v50, v51\n\t"
    "v_mfma_f32_16x16x32_bf16 v[196:199], a[168:171], v[32:35], v[196:199]\n\t"
    "s_waitcnt lgkmcnt(1)\n\t"
    "buffer_store_dwordx4 v[20:23], v9, s[48:51], s63 offen offset:1024 sc1\n\t"
    "v_accvgpr_read_b32 v14, a240\n\t"
    "v_mfma_f32_16x16x32_bf16 v[196:199], a[172:175], v[36:39], v[196:199]\n\t"
    "v_exp_f32 v52, v52\n\t"
    "v_mfma_f32_16x16x32_bf16 v[200:203], a[176:179], v[32:35], v[200:203]\n\t"
    "v_exp_f32 v53, v53\n\t"
    "ds_read_b128 v[20:23], v8 offset:16320\n\t"
    "v_mfma_f32_16x16x32_bf16 v[200:203], a[180:183], v[36:39], v[200:203]\n\t"
    "v_exp_f32 v54, v54\n\t"
    "v_mfma_f32_16x16x32_bf16 v[204:207], a[184:187], v[32:35], v[204:207]\n\t"
    "v_exp_f32 v55, v55\n\t"
    "s_waitcnt lgkmcnt(1)\n\t"
    "buffer_store_dwordx4 v[16:19], v9, s[48:51], s63 offen offset:2048 sc1\n\t"
    "v_mfma_f32_16x16x32_bf16 v[204:207], a[188:191], v[36:39], v[204:207]\n\t"
    "v_cvt_pk_bf16_f32 v42, v52, v53\n\t"
    "v_cvt_pk_bf16_f32 v43, v54, v55\n\t"
    "v_mfma_f32_16x16x32_bf16 v[208:211], a[192:195], v[32:35], v[208:211]\n\t"
    "v_exp_f32 v56, v56\n\t"
    "v_mfma_f32_16x16x32_bf16 v[208:211], a[196:199], v[36:39], v[208:211]\n\t"
    "v_exp_f32 v57, v57\n\t"
    "v_accvgpr_read_b32 v15, a241\n\t"
    "v_add_f32 v12, v14, v15\n\t"
    "v_subrev_u32 v14, s54, v12\n\t"
    "v_max_u32 v10, v10, v14\n\t"
    "v_rcp_f32 v12, v12\n\t"
    "s_nop 0\n\t"
    "v_mul_f32 v192, v12, v192\n\t"
    "v_mul_f32 v193, v12, v193\n\t"
    "v_mul_f32 v194, v12, v194\n\t"
    "v_mul_f32 v195, v12, v195\n\t"
    "v_cvt_pk_bf16_f32 v192, v192, v193\n\t"
    "v_cvt_pk_bf16_f32 v193, v194, v195\n\t"
    "ds_write_b64 v7, v[192:193] offset:17408\n\t"
    "v_mfma_f32_16x16x32_bf16 v[212:215], a[200:203], v[32:35], v[212:215]\n\t"
    "v_exp_f32 v58, v58\n\t"
    "s_waitcnt lgkmcnt(1)\n\t"
    "buffer_store_dwordx4 v[20:23], v9, s[48:51], s63 offen offset:3072 sc1\n\t"
    "v_mul_f32 v196, v12, v196\n\t"
    "v_mul_f32 v197, v12, v197\n\t"
    "v_mul_f32 v198, v12, v198\n\t"
    "v_mul_f32 v199, v12, v199\n\t"
    "v_cvt_pk_bf16_f32 v196, v196, v197\n\t"
    "v_cvt_pk_bf16_f32 v197, v198, v199\n\t"
    "ds_write_b64 v7, v[196:197] offset:17440\n\t"
    "v_mfma_f32_16x16x32_bf16 v[212:215], a[204:207], v[36:39], v[212:215]\n\t"
    "s_add_u32 s64, s62, 16384\n\t"
    "v_exp_f32 v59, v59\n\t"
    "v_mfma_f32_16x16x32_bf16 v[216:219], a[208:211], v[32:35], v[216:219]\n\t"
    "v_cvt_pk_bf16_f32 v44, v56, v57\n\t"
    "v_cvt_pk_bf16_f32 v45, v58, v59\n\t"
    "v_mul_f32 v200, v12, v200\n\t"
    "v_mul_f32 v201, v12, v201\n\t"
    "v_mul_f32 v202, v12, v202\n\t"
    "v_mul_f32 v203, v12, v203\n\t"
    "v_cvt_pk_bf16_f32 v200, v200, v201\n\t"
    "v_cvt_pk_bf16_f32 v201, v202, v203\n\t"
    "ds_write_b64 v7, v[200:201] offset:17472\n\t"
    "v_mfma_f32_16x16x32_bf16 v[216:219], a[212:215], v[36:39], v[216:219]\n\t"
    "v_exp_f32 v60, v60\n\t"
    "v_exp_f32 v61, v61\n\t"
    "v_exp_f32 v62, v62\n\t"
    "v_exp_f32 v63, v63\n\t"
    "v_mfma_f32_16x16x32_bf16 v[220:223], a[216:219], v[32:35], v[220:223]\n\t"
    "v_mul_f32 v204, v12, v204\n\t"
    "v_mul_f32 v205, v12, v205\n\t"
    "v_mul_f32 v206, v12, v206\n\t"
    "v_mul_f32 v207, v12, v207\n\t"
    "v_cvt_pk_bf16_f32 v204, v204, v205\n\t"
    "v_cvt_pk_bf16_f32 v205, v206, v207\n\t"
    "ds_write_b64 v7, v[204:205] offset:17504\n\t"
    "v_cvt_pk_bf16_f32 v46, v60, v61\n\t"
    "v_cvt_pk_bf16_f32 v47, v62, v63\n\t"
    "v_mfma_f32_16x16x32_bf16 v[220:223], a[220:223], v[36:39], v[220:223]\n\t"
    "v_mul_f32 v208, v12, v208\n\t"
    "v_mul_f32 v209, v12, v209\n\t"
    "v_smfmac_f32_16x16x64_bf16 a[244:247], a[248:251], v[40:47], v3\n\t"
    "v_mul_f32 v210, v12, v210\n\t"
    "v_mul_f32 v211, v12, v211\n\t"
    "v_cvt_pk_bf16_f32 v208, v208, v209\n\t"
    "v_cvt_pk_bf16_f32 v209, v210, v211\n\t"
    "ds_write_b64 v7, v[208:209] offset:17536\n\t"
    "v_mfma_f32_16x16x32_bf16 v[224:227], a[160:163], v[40:43], v[224:227]\n\t"
    "v_mul_f32 v212, v12, v212\n\t"
    "v_mul_f32 v213, v12, v213\n\t"
    "v_mfma_f32_16x16x32_bf16 v[224:227], a[164:167], v[44:47], v[224:227]\n\t"
    "v_mul_f32 v214, v12, v214\n\t"
    "v_mul_f32 v215, v12, v215\n\t"
    "v_cvt_pk_bf16_f32 v212, v212, v213\n\t"
    "v_cvt_pk_bf16_f32 v213, v214, v215\n\t"
    "ds_write_b64 v7, v[212:213] offset:17568\n\t"
    "v_mfma_f32_16x16x32_bf16 v[228:231], a[168:171], v[40:43], v[228:231]\n\t"
    "v_mul_f32 v216, v12, v216\n\t"
    "v_mul_f32 v217, v12, v217\n\t"
    "v_mfma_f32_16x16x32_bf16 v[228:231], a[172:175], v[44:47], v[228:231]\n\t"
    "v_mul_f32 v218, v12, v218\n\t"
    "v_mul_f32 v219, v12, v219\n\t"
    "v_cvt_pk_bf16_f32 v216, v216, v217\n\t"
    "v_cvt_pk_bf16_f32 v217, v218, v219\n\t"
    "ds_write_b64 v7, v[216:217] offset:17600\n\t"
    "v_mfma_f32_16x16x32_bf16 v[232:235], a[176:179], v[40:43], v[232:235]\n\t"
    "v_accvgpr_read_b32 v14, a244\n\t"
    "v_accvgpr_read_b32 v15, a245\n\t"
    "v_add_f32 v13, v14, v15\n\t"
    "v_subrev_u32 v14, s54, v13\n\t"
    "v_max_u32 v10, v10, v14\n\t"
    "v_rcp_f32 v13, v13\n\t"
    "v_mfma_f32_16x16x32_bf16 v[232:235], a[180:183], v[44:47], v[232:235]\n\t"
    "v_mul_f32 v220, v12, v220\n\t"
    "v_mul_f32 v221, v12, v221\n\t"
    "v_mul_f32 v222, v12, v222\n\t"
    "v_mul_f32 v223, v12, v223\n\t"
    "v_cvt_pk_bf16_f32 v220, v220, v221\n\t"
    "v_cvt_pk_bf16_f32 v221, v222, v223\n\t"
    "ds_write_b64 v7, v[220:221] offset:17632\n\t"
    "v_mul_f32 v224, v13, v224\n\t"
    "v_mul_f32 v225, v13, v225\n\t"
    "v_mul_f32 v226, v13, v226\n\t"
    "v_mul_f32 v227, v13, v227\n\t"
    "v_cvt_pk_bf16_f32 v224, v224, v225\n\t"
    "v_cvt_pk_bf16_f32 v225, v226, v227\n\t"
    "ds_write_b64 v7, v[224:225] offset:0\n\t"
    "v_mfma_f32_16x16x32_bf16 v[236:239], a[184:187], v[40:43], v[236:239]\n\t"
    "ds_read_b128 v[16:19], v8 offset:17408\n\t"
    "v_mul_f32 v228, v13, v228\n\t"
    "v_mfma_f32_16x16x32_bf16 v[236:239], a[188:191], v[44:47], v[236:239]\n\t"
    "ds_read_b128 v[20:23], v8 offset:18496\n\t"
    "v_mul_f32 v229, v13, v229\n\t"
    "v_mul_f32 v230, v13, v230\n\t"
    "v_mul_f32 v231, v13, v231\n\t"
    "v_cvt_pk_bf16_f32 v228, v228, v229\n\t"
    "v_cvt_pk_bf16_f32 v229, v230, v231\n\t"
    "ds_write_b64 v7, v[228:229] offset:32\n\t"
    "v_mfma_f32_16x16x32_bf16 v[240:243], a[192:195], v[40:43], v[240:243]\n\t"
    "v_mul_f32 v232, v13, v232\n\t"
    "v_mul_f32 v233, v13, v233\n\t"
    "v_mfma_f32_16x16x32_bf16 v[240:243], a[196:199], v[44:47], v[240:243]\n\t"
    "v_mul_f32 v234, v13, v234\n\t"
    "v_mul_f32 v235, v13, v235\n\t"
    "v_cvt_pk_bf16_f32 v232, v232, v233\n\t"
    "v_cvt_pk_bf16_f32 v233, v234, v235\n\t"
    "ds_write_b64 v7, v[232:233] offset:64\n\t"
    "v_mfma_f32_16x16x32_bf16 v[244:247], a[200:203], v[40:43], v[244:247]\n\t"
    "v_mul_f32 v236, v13, v236\n\t"
    "v_mul_f32 v237, v13, v237\n\t"
    "v_mfma_f32_16x16x32_bf16 v[244:247], a[204:207], v[44:47], v[244:247]\n\t"
    "v_mul_f32 v238, v13, v238\n\t"
    "v_mul_f32 v239, v13, v239\n\t"
    "v_cvt_pk_bf16_f32 v236, v236, v237\n\t"
    "v_cvt_pk_bf16_f32 v237, v238, v239\n\t"
    "ds_write_b64 v7, v[236:237] offset:96\n\t"
    "v_mfma_f32_16x16x32_bf16 v[248:251], a[208:211], v[40:43], v[248:251]\n\t"
    "s_waitcnt lgkmcnt(4)\n\t"
    "buffer_store_dwordx4 v[16:19], v9, s[48:51], s64 offen offset:0 sc1\n\t"
    "v_mul_f32 v240, v13, v240\n\t"
    "v_mfma_f32_16x16x32_bf16 v[248:251], a[212:215], v[44:47], v[248:251]\n\t"
    "ds_read_b128 v[16:19], v8 offset:19584\n\t"
    "v_mul_f32 v241, v13, v241\n\t"
    "v_mul_f32 v242, v13, v242\n\t"
    "v_mul_f32 v243, v13, v243\n\t"
    "v_cvt_pk_bf16_f32 v240, v240, v241\n\t"
    "v_cvt_pk_bf16_f32 v241, v242, v243\n\t"
    "ds_write_b64 v7, v[240:241] offset:128\n\t"
    "v_mfma_f32_16x16x32_bf16 v[252:255], a[216:219], v[40:43], v[252:255]\n\t"
    "s_waitcnt lgkmcnt(5)\n\t"
    "buffer_store_dwordx4 v[20:23], v9, s[48:51], s64 offen offset:1024 sc1\n\t"
    "v_mul_f32 v244, v13, v244\n\t"
    "v_mfma_f32_16x16x32_bf16 v[252:255], a[220:223], v[44:47], v[252:255]\n\t"
    "ds_read_b128 v[20:23], v8 offset:20672\n\t"
    "v_mul_f32 v245, v13, v245\n\t"
    "v_mul_f32 v246, v13, v246\n\t"
    "v_mul_f32 v247, v13, v247\n\t"
    "v_cvt_pk_bf16_f32 v244, v244, v245\n\t"
    "v_cvt_pk_bf16_f32 v245, v246, v247\n\t"
    "ds_write_b64 v7, v[244:245] offset:160\n\t"
    "s_waitcnt lgkmcnt(3)\n\t"
    "buffer_store_dwordx4 v[16:19], v9, s[48:51], s64 offen offset:2048 sc1\n\t"
    "v_mul_f32 v248, v13, v248\n\t"
    "v_cmp_lt_u32 vcc, s55, v10\n\t"
    "s_waitcnt lgkmcnt(1)\n\t"
    "buffer_store_dwordx4 v[20:23], v9, s[48:51], s64 offen offset:3072 sc1\n\t"
    "v_mul_f32 v249, v13, v249\n\t"
    "s_add_u32 s63, s62, 20480\n\t"
    "v_mul_f32 v250, v13, v250\n\t"
    "v_mul_f32 v251, v13, v251\n\t"
    "v_cvt_pk_bf16_f32 v248, v248, v249\n\t"
    "v_cvt_pk_bf16_f32 v249, v250, v251\n\t"
    "ds_write_b64 v7, v[248:249] offset:192\n\t"
    "v_mul_f32 v252, v13, v252\n\t"
    "v_mul_f32 v253, v13, v253\n\t"
    "v_mul_f32 v254, v13, v254\n\t"
    "v_mul_f32 v255, v13, v255\n\t"
    "v_cvt_pk_bf16_f32 v252, v252, v253\n\t"
    "v_cvt_pk_bf16_f32 v253, v254, v255\n\t"
    "ds_write_b64 v7, v[252:253] offset:224\n\t"
    "ds_read_b128 v[16:19], v8 offset:0\n\t"
    "ds_read_b128 v[20:23], v8 offset:1088\n\t"
    "s_waitcnt lgkmcnt(1)\n\t"
    "buffer_store_dwordx4 v[16:19], v9, s[48:51], s63 offen offset:0 sc1\n\t"
    "s_nop 1\n\t"
    "ds_read_b128 v[16:19], v8 offset:2176\n\t"
    "s_waitcnt lgkmcnt(1)\n\t"
    "buffer_store_dwordx4 v[20:23], v9, s[48:51], s63 offen offset:1024 sc1\n\t"
    "s_nop 1\n\t"
    "ds_read_b128 v[20:23], v8 offset:3264\n\t"
    "s_waitcnt lgkmcnt(1)\n\t"
    "buffer_store_dwordx4 v[16:19], v9, s[48:51], s63 offen offset:2048 sc1\n\t"
    "s_waitcnt lgkmcnt(0)\n\t"
    "buffer_store_dwordx4 v[20:23], v9, s[48:51], s63 offen offset:3072 sc1\n\t"
    "s_nop 4\n\t"
    "s_or_b32 s66, vcc_lo, vcc_hi\n\t"
    "s_cmp_lg_u32 s66, 0\n\t"
    "s_cselect_b32 s66, 2, 0\n\t"
    "s_cmp_lg_u32 s65, 0\n\t"
    "s_cselect_b32 s65, 1, 0\n\t"
    "s_or_b32 %[flags], s65, s66\n\t"
    "s_cmp_lg_u32 %[flags], 0\n\t"
    "s_cbranch_scc1 Lslow\n\t"
    "s_endpgm\n\t"
    "Lslow:\n\t"
    "Lend:\n\t"
    : [flags] "=&s"(flags), [qp] "=&s"(qp), [kp] "=&s"(kp), [vp] "=&s"(vp), [op] "=&s"(op)
    : [ka] "s"(ka), [bid] "s"(bid), [wv] "s"(wv)
    : "memory", "vcc", "scc", "v0", "v1", "v2", "v3", "v4", "v5", "v6", "v7", "v8", "v9", "v10", "v11", "v12", "v13", "v14", "v15", "v16", "v17", "v18", "v19", "v20", "v21", "v22", "v23", "v24", "v25", "v26", "v27", "v28", "v29", "v30", "v31", "v32", "v33", "v34", "v35", "v36", "v37", "v38", "v39", "v40", "v41", "v42", "v43", "v44", "v45", "v46", "v47", "v48", "v49", "v50", "v51", "v52", "v53", "v54", "v55", "v56", "v57", "v58", "v59", "v60", "v61", "v62", "v63", "v64", "v65", "v66", "v67", "v68", "v69", "v70", "v71", "v72", "v73", "v74", "v75", "v76", "v77", "v78", "v79", "v80", "v81", "v82", "v83", "v84", "v85", "v86", "v87", "v88", "v89", "v90", "v91", "v92", "v93", "v94", "v95", "v96", "v97", "v98", "v99", "v100", "v101", "v102", "v103", "v104", "v105", "v106", "v107", "v108", "v109", "v110", "v111", "v112", "v113", "v114", "v115", "v116", "v117", "v118", "v119", "v120", "v121", "v122", "v123", "v124", "v125", "v126", "v127", "v128", "v129", "v130", "v131", "v132", "v133", "v134", "v135", "v136", "v137", "v138", "v139", "v140", "v141", "v142", "v143", "v144", "v145", "v146", "v147", "v148", "v149", "v150", "v151", "v152", "v153", "v154", "v155", "v156", "v157", "v158", "v159", "v160", "v161", "v162", "v163", "v164", "v165", "v166", "v167", "v168", "v169", "v170", "v171", "v172", "v173", "v174", "v175", "v176", "v177", "v178", "v179", "v180", "v181", "v182", "v183", "v184", "v185", "v186", "v187", "v188", "v189", "v190", "v191", "v192", "v193", "v194", "v195", "v196", "v197", "v198", "v199", "v200", "v201", "v202", "v203", "v204", "v205", "v206", "v207", "v208", "v209", "v210", "v211", "v212", "v213", "v214", "v215", "v216", "v217", "v218", "v219", "v220", "v221", "v222", "v223", "v224", "v225", "v226", "v227", "v228", "v229", "v230", "v231", "v232", "v233", "v234", "v235", "v236", "v237", "v238", "v239", "v240", "v241", "v242", "v243", "v244", "v245", "v246", "v247", "v248", "v249", "v250", "v251", "v252", "v253", "v254", "v255", "a0", "a1", "a2", "a3", "a4", "a5", "a6", "a7", "a8", "a9", "a10", "a11", "a12", "a13", "a14", "a15", "a16", "a17", "a18", "a19", "a20", "a21", "a22", "a23", "a24", "a25", "a26", "a27", "a28", "a29", "a30", "a31", "a32", "a33", "a34", "a35", "a36", "a37", "a38", "a39", "a40", "a41", "a42", "a43", "a44", "a45", "a46", "a47", "a48", "a49", "a50", "a51", "a52", "a53", "a54", "a55", "a56", "a57", "a58", "a59", "a60", "a61", "a62", "a63", "a64", "a65", "a66", "a67", "a68", "a69", "a70", "a71", "a72", "a73", "a74", "a75", "a76", "a77", "a78", "a79", "a80", "a81", "a82", "a83", "a84", "a85", "a86", "a87", "a88", "a89", "a90", "a91", "a92", "a93", "a94", "a95", "a96", "a97", "a98", "a99", "a100", "a101", "a102", "a103", "a104", "a105", "a106", "a107", "a108", "a109", "a110", "a111", "a112", "a113", "a114", "a115", "a116", "a117", "a118", "a119", "a120", "a121", "a122", "a123", "a124", "a125", "a126", "a127", "a128", "a129", "a130", "a131", "a132", "a133", "a134", "a135", "a136", "a137", "a138", "a139", "a140", "a141", "a142", "a143", "a144", "a145", "a146", "a147", "a148", "a149", "a150", "a151", "a152", "a153", "a154", "a155", "a156", "a157", "a158", "a159", "a160", "a161", "a162", "a163", "a164", "a165", "a166", "a167", "a168", "a169", "a170", "a171", "a172", "a173", "a174", "a175", "a176", "a177", "a178", "a179", "a180", "a181", "a182", "a183", "a184", "a185", "a186", "a187", "a188", "a189", "a190", "a191", "a192", "a193", "a194", "a195", "a196", "a197", "a198", "a199", "a200", "a201", "a202", "a203", "a204", "a205", "a206", "a207", "a208", "a209", "a210", "a211", "a212", "a213", "a214", "a215", "a216", "a217", "a218", "a219", "a220", "a221", "a222", "a223", "a224", "a225", "a226", "a227", "a228", "a229", "a230", "a231", "a232", "a233", "a234", "a235", "a236", "a237", "a238", "a239", "a240", "a241", "a242", "a243", "a244", "a245", "a246", "a247", "a248", "a249", "a250", "a251", "a252", "a253", "a254", "a255", "s36", "s37", "s38", "s39", "s40", "s41", "s42", "s43", "s44", "s45", "s46", "s47", "s48", "s49", "s50", "s51", "s52", "s53", "s54", "s55", "s56", "s57", "s58", "s59", "s60", "s61", "s62", "s63", "s64", "s65", "s66", "s67", "s68", "s69", "s72", "s73", "s74", "s75", "s76", "s77", "s78", "s79", "s80", "s81", "s82", "s83", "s84", "s85", "s86", "s87");
  if (flags) {
    asm volatile("s_waitcnt vmcnt(0) lgkmcnt(0)" ::: "memory");
    const unsigned grp = (bid & 7u) * 16u + (bid >> 4), half = (bid >> 3) & 1u;
    for (int b = 0; b < 2; ++b)
      if (flags & (1u << b))
        exact_rows(qp, kp + (long)grp * 65536, vp + (long)grp * 65536, op, (long)grp * 1536 + half * 768 + b * 384 + wv * 96);
  }
}

extern "C" void kernel_launch(void* const* d_in, const int* in_sizes, int n_in, void* d_out, int out_size,
                              void* d_ws, size_t ws_size, hipStream_t stream) {
  (void)in_sizes; (void)n_in; (void)out_size; (void)d_ws; (void)ws_size;
  (void)hipFuncSetAttribute((const void*)gqa_fwd, hipFuncAttributeMaxDynamicSharedMemorySize, 163840);
  hipLaunchKernelGGL(gqa_fwd, dim3(256), dim3(256), 163840, stream,
                     (const unsigned short*)d_in[0], (const unsigned short*)d_in[1], (const unsigned short*)d_in[2], (unsigned short*)d_out);
}
```
